```python
import torch

def _softmax(x, axis=-1):
    return torch.softmax(x, dim=axis)
def _gelu(x):
    return torch.nn.functional.gelu(x, approximate="tanh")
def _silu(x):
    return torch.nn.functional.silu(x)
def _sigmoid(x):
    return torch.sigmoid(x)
def _layernorm(x, axis=-1, eps=1e-5):
    m = x.mean(dim=axis, keepdim=True)
    v = x.var(dim=axis, keepdim=True, unbiased=False)
    return (x - m) / torch.sqrt(v + eps)
def _scatter_add(tgt, idx, src, axis=0):
    return tgt.clone().index_add_(axis, idx, src)
def _segment_sum(src, seg, n):
    out = torch.zeros((n,) + src.shape[1:], dtype=src.dtype)
    return out.index_add_(0, seg, src)
def _pad(x, pw):
    flat = tuple(v for pair in reversed(pw) for v in pair)
    return torch.nn.functional.pad(x, flat)

kernel_name = 'sequential_preLN__spectral_gfnet__mixer_channel_mlp__prompt'

def setup_inputs():

    _w = (lambda a: (lambda t: t.to(torch.bfloat16) if t.is_floating_point() else t)(torch.as_tensor(a)))
    def _A_setup():
        B, S, D = 1, 2048, 640
        Df = D // 2 + 1
        return {
            "x": _w((lambda *s: torch.randn(*s, dtype=torch.bfloat16) * 0.05)(B, S, D)),
            "w_r": _w((lambda *s: torch.randn(*s, dtype=torch.bfloat16) * 0.8)(S, Df) * 0.02),
            "w_i": _w((lambda *s: torch.randn(*s, dtype=torch.bfloat16) * 0.8)(S, Df) * 0.02),
        }
    _A_in = _A_setup()

    def _B_setup():
        B, S, D, F = 1, 2048, 640, 1728
        return {
            "x": _w((lambda *s: torch.randn(*s, dtype=torch.bfloat16) * 0.05)(B, S, D)),
            "w1": _w((lambda *s: torch.randn(*s, dtype=torch.bfloat16) * 0.8)(D, F) * (D ** -0.5)),
            "b1": _w((lambda *s: torch.randn(*s, dtype=torch.bfloat16) * 0.4)(F)),
            "w2": _w((lambda *s: torch.randn(*s, dtype=torch.bfloat16) * 0.05)(F, D) * (F ** -0.5)),
            "b2": _w((lambda *s: torch.randn(*s, dtype=torch.bfloat16) * 0.05)(D)),
        }
    _B_in = _B_setup()

    return {
        "A_x": _A_in["x"],
        "A_w_r": _A_in["w_r"],
        "A_w_i": _A_in["w_i"],
        "B_x": _B_in["x"],
        "B_w1": _B_in["w1"],
        "B_b1": _B_in["b1"],
        "B_w2": _B_in["w2"],
        "B_b2": _B_in["b2"],
    }

def reference(A_x, A_w_r, A_w_i, B_x, B_w1, B_b1, B_w2, B_b2):
    def _A_ref(x, w_r, w_i):
        B, S, D = x.shape
        X = (lambda x, axes=(-2,-1): torch.fft.rfft2(x.float(), dim=axes))(x, axes=(-2, -1))
        W = w_r + 1j * w_i
        X = X * W
        y = (lambda x, s, axes=(-2,-1): torch.fft.irfft2(x, s=s, dim=axes).to(torch.bfloat16))(X, (S, D), axes=(-2, -1))
        return y

    def _B_ref(x, w1, b1, w2, b2):
        y = _layernorm(x, axis=-1)
        h = _gelu(torch.einsum("bsd,df->bsf", y, w1) + b1)
        return torch.einsum("bsf,fd->bsd", h, w2) + b2

    x = A_x
    h = x + _A_ref(_layernorm(x, axis=-1), A_w_r, A_w_i)
    h2 = h + _B_ref(_layernorm(h, axis=-1), B_w1, B_b1, B_w2, B_b2)
    return h2
```

```cpp
#include <hip/hip_runtime.h>
#include <hip/hip_bf16.h>
#include <stdint.h>


#define NS 2048
#define ND 640
#define NKF 321
#define NF 1728

typedef __attribute__((ext_vector_type(8))) short bf16x8_t;
typedef __attribute__((ext_vector_type(4))) float f32x4_t;

__device__ __forceinline__ float2 cadd(float2 a, float2 b) { return make_float2(a.x + b.x, a.y + b.y); }
__device__ __forceinline__ float2 csub(float2 a, float2 b) { return make_float2(a.x - b.x, a.y - b.y); }
__device__ __forceinline__ float2 cmul(float2 a, float2 b) {
    return make_float2(a.x * b.x - a.y * b.y, a.x * b.y + a.y * b.x);
}
__device__ __forceinline__ float bfr(float f) { return __bfloat162float(__float2bfloat16(f)); }

__device__ __forceinline__ float wave_sum(float v) {
#pragma unroll
    for (int o = 32; o > 0; o >>= 1) v += __shfl_xor(v, o, 64);
    return v;
}

template <int S>
__device__ __forceinline__ float2 rot90(float2 v) { return make_float2(-(float)S * v.y, (float)S * v.x); }

template <int S>
__device__ __forceinline__ void dft4(float2& v0, float2& v1, float2& v2, float2& v3) {
    float2 a = cadd(v0, v2), b = csub(v0, v2), c = cadd(v1, v3), d = rot90<S>(csub(v1, v3));
    v0 = cadd(a, c); v2 = csub(a, c); v1 = cadd(b, d); v3 = csub(b, d);
}

template <int R, int S> struct DFT;

template <int S> struct DFT<4, S> {
    static __device__ __forceinline__ void run(float2* v) { dft4<S>(v[0], v[1], v[2], v[3]); }
};

template <int S> struct DFT<8, S> {
    static __device__ __forceinline__ void run(float2* v) {
        float2 e0 = v[0], e1 = v[2], e2 = v[4], e3 = v[6];
        float2 o0 = v[1], o1 = v[3], o2 = v[5], o3 = v[7];
        dft4<S>(e0, e1, e2, e3);
        dft4<S>(o0, o1, o2, o3);
        const float c = 0.70710678118654752f;
        o1 = make_float2(c * (o1.x - (float)S * o1.y), c * (o1.y + (float)S * o1.x));
        o2 = rot90<S>(o2);
        o3 = make_float2(c * (-o3.x - (float)S * o3.y), c * (-o3.y + (float)S * o3.x));
        v[0] = cadd(e0, o0); v[4] = csub(e0, o0);
        v[1] = cadd(e1, o1); v[5] = csub(e1, o1);
        v[2] = cadd(e2, o2); v[6] = csub(e2, o2);
        v[3] = cadd(e3, o3); v[7] = csub(e3, o3);
    }
};

template <int S>
__device__ __forceinline__ void dft5(float2* v) {
    const float c1 = 0.30901699437494742f, c2 = -0.80901699437494742f;
    const float s1 = 0.95105651629515357f, s2 = 0.58778525229247313f;
    float2 t1 = cadd(v[1], v[4]), t2 = cadd(v[2], v[3]);
    float2 t3 = csub(v[1], v[4]), t4 = csub(v[2], v[3]);
    float2 a1 = make_float2(v[0].x + c1 * t1.x + c2 * t2.x, v[0].y + c1 * t1.y + c2 * t2.y);
    float2 a2 = make_float2(v[0].x + c2 * t1.x + c1 * t2.x, v[0].y + c2 * t1.y + c1 * t2.y);
    float2 b1 = make_float2((float)S * (s1 * t3.x + s2 * t4.x), (float)S * (s1 * t3.y + s2 * t4.y));
    float2 b2 = make_float2((float)S * (s2 * t3.x - s1 * t4.x), (float)S * (s2 * t3.y - s1 * t4.y));
    float2 X0 = make_float2(v[0].x + t1.x + t2.x, v[0].y + t1.y + t2.y);
    v[1] = make_float2(a1.x - b1.y, a1.y + b1.x);
    v[4] = make_float2(a1.x + b1.y, a1.y - b1.x);
    v[2] = make_float2(a2.x - b2.y, a2.y + b2.x);
    v[3] = make_float2(a2.x + b2.y, a2.y - b2.x);
    v[0] = X0;
}

template <int S> struct DFT<10, S> {
    static __device__ __forceinline__ void run(float2* v) {
        float2 e[5] = {v[0], v[2], v[4], v[6], v[8]};
        float2 o[5] = {v[1], v[3], v[5], v[7], v[9]};
        dft5<S>(e);
        dft5<S>(o);
        o[1] = cmul(o[1], make_float2(0.80901699437494742f, (float)S * 0.58778525229247313f));
        o[2] = cmul(o[2], make_float2(0.30901699437494742f, (float)S * 0.95105651629515357f));
        o[3] = cmul(o[3], make_float2(-0.30901699437494742f, (float)S * 0.95105651629515357f));
        o[4] = cmul(o[4], make_float2(-0.80901699437494742f, (float)S * 0.58778525229247313f));
#pragma unroll
        for (int k = 0; k < 5; ++k) {
            v[k] = cadd(e[k], o[k]);
            v[k + 5] = csub(e[k], o[k]);
        }
    }
};

template <int R, int S>
__device__ __forceinline__ void stockham_pass(const float2* in, float2* out, int j, int N, int Ns,
                                              const float2* tw) {
    float2 v[R];
    const int q = N / R;
#pragma unroll
    for (int r = 0; r < R; ++r) v[r] = in[j + r * q];
    if (Ns > 1) {
        const int jm = j % Ns;
        const int step = N / (Ns * R);
#pragma unroll
        for (int r = 1; r < R; ++r) {
            float2 w = tw[jm * r * step];
            w.y *= -(float)S;
            v[r] = cmul(v[r], w);
        }
    }
    DFT<R, S>::run(v);
    const int o = (j / Ns) * Ns * R + (j % Ns);
#pragma unroll
    for (int r = 0; r < R; ++r) out[o + r * Ns] = v[r];
}

__device__ __forceinline__ void ln_row_regs(const float (&v)[10], float (&o)[10]) {
    float s = 0.f;
#pragma unroll
    for (int i = 0; i < 10; ++i) s += v[i];
    s = wave_sum(s);
    const float meanf = s * (1.0f / ND);
    float d2 = 0.f;
#pragma unroll
    for (int i = 0; i < 10; ++i) { float d = v[i] - meanf; d2 = fmaf(d, d, d2); }
    d2 = wave_sum(d2);
    const float mean_b = bfr(meanf);
    const float varb = bfr(d2 * (1.0f / ND));
    const float sd = bfr(sqrtf(bfr(varb + 1e-5f)));
#pragma unroll
    for (int i = 0; i < 10; ++i) o[i] = bfr(bfr(v[i] - mean_b) / sd);
}

#define S1_FFT_BLOCKS (NS / 8)
#define TR_W1_TILES ((ND / 64) * (NF / 64))
#define TR_W2_TILES ((NF / 64) * (ND / 64))

__device__ __forceinline__ void transpose_tile(unsigned char* smem, const unsigned short* src,
                                               unsigned short* dst, int R, int C, int tr, int tc) {
    unsigned short* tile = (unsigned short*)smem;
    for (int i = threadIdx.x; i < 4096; i += 256) {
        int r = i >> 6, c = i & 63;
        tile[r * 66 + c] = src[(size_t)(tr * 64 + r) * C + tc * 64 + c];
    }
    __syncthreads();
    for (int i = threadIdx.x; i < 4096; i += 256) {
        int r = i >> 6, c = i & 63;
        dst[(size_t)(tc * 64 + r) * R + tr * 64 + c] = tile[c * 66 + r];
    }
}

#define SMEM_FFT640 ((ND + 2 * 4 * ND) * 8)

__global__ __launch_bounds__(256) void k_stage1(const __hip_bfloat16* __restrict__ x, float2* __restrict__ spec,
                                                const unsigned short* __restrict__ w1,
                                                unsigned short* __restrict__ w1t,
                                                const unsigned short* __restrict__ w2,
                                                unsigned short* __restrict__ w2t) {
    __shared__ __attribute__((aligned(16))) unsigned char smem[SMEM_FFT640];
    const int tid = threadIdx.x;
    int bid = blockIdx.x;
    if (bid >= S1_FFT_BLOCKS) {
        bid -= S1_FFT_BLOCKS;
        if (bid < TR_W1_TILES) {
            transpose_tile(smem, w1, w1t, ND, NF, bid / (NF / 64), bid % (NF / 64));
        } else {
            bid -= TR_W1_TILES;
            transpose_tile(smem, w2, w2t, NF, ND, bid / (ND / 64), bid % (ND / 64));
        }
        return;
    }
    float2* tw = (float2*)smem;
    float2* bufA = tw + ND;
    float2* bufB = bufA + 4 * ND;
    const int s0 = bid * 8;
    for (int m = tid; m < ND; m += 256) {
        float sn, cs;
        sincospif((float)m * (1.0f / 320.0f), &sn, &cs);
        tw[m] = make_float2(cs, -sn);
    }
    const int wave = tid >> 6, lane = tid & 63;
    for (int rr = 0; rr < 2; ++rr) {
        const int row = s0 + wave * 2 + rr;
        const __hip_bfloat16* xr = x + (size_t)row * ND;
        float v[10], o[10];
#pragma unroll
        for (int i = 0; i < 10; ++i) v[i] = __bfloat162float(xr[lane + 64 * i]);
        ln_row_regs(v, o);
        float* dst = (float*)(bufA + wave * ND);
#pragma unroll
        for (int i = 0; i < 10; ++i) dst[(lane + 64 * i) * 2 + rr] = o[i];
    }
    __syncthreads();
    for (int j = tid; j < 4 * (ND / 8); j += 256) {
        const int f = j / (ND / 8), jj = j % (ND / 8);
        stockham_pass<8, -1>(bufA + f * ND, bufB + f * ND, jj, ND, 1, tw);
    }
    __syncthreads();
    for (int j = tid; j < 4 * (ND / 8); j += 256) {
        const int f = j / (ND / 8), jj = j % (ND / 8);
        stockham_pass<8, -1>(bufB + f * ND, bufA + f * ND, jj, ND, 8, tw);
    }
    __syncthreads();
    for (int j = tid; j < 4 * (ND / 10); j += 256) {
        const int f = j / (ND / 10), jj = j % (ND / 10);
        stockham_pass<10, -1>(bufA + f * ND, bufB + f * ND, jj, ND, 64, tw);
    }
    __syncthreads();
    for (int it = tid; it < NKF * 8; it += 256) {
        const int k = it >> 3, sl = it & 7;
        const int f = sl >> 1, part = sl & 1;
        const float2 zk = bufB[f * ND + k];
        const float2 zn = bufB[f * ND + ((ND - k) % ND)];
        float2 val;
        if (part == 0) val = make_float2(0.5f * (zk.x + zn.x), 0.5f * (zk.y - zn.y));
        else val = make_float2(0.5f * (zk.y + zn.y), -0.5f * (zk.x - zn.x));
        spec[(size_t)k * NS + s0 + sl] = val;
    }
}

__global__ __launch_bounds__(256) void k_stage2(float2* __restrict__ spec, const __hip_bfloat16* __restrict__ wr,
                                                const __hip_bfloat16* __restrict__ wi) {
    __shared__ __attribute__((aligned(16))) float2 sm[3 * NS];
    float2* tw = sm;
    float2* bA = sm + NS;
    float2* bB = sm + 2 * NS;
    const int tid = threadIdx.x;
    const int orig = blockIdx.x;
    const int q = NKF / 8, r8 = NKF % 8, xcd = orig % 8;
    const int k = (xcd < r8 ? xcd * (q + 1) : r8 * (q + 1) + (xcd - r8) * q) + orig / 8;
    float2* col = spec + (size_t)k * NS;

    float2 v[8];
#pragma unroll
    for (int r = 0; r < 8; ++r) v[r] = col[tid + 256 * r];
    float2 wv[8];
#pragma unroll
    for (int b = 0; b < 2; ++b)
#pragma unroll
        for (int r = 0; r < 4; ++r) {
            const int s = tid + 256 * b + 512 * r;
            wv[b * 4 + r] = make_float2(__bfloat162float(wr[(size_t)s * NKF + k]),
                                        __bfloat162float(wi[(size_t)s * NKF + k]));
        }
    for (int m = tid; m < NS; m += 256) {
        float sn, cs;
        sincospif((float)m * (1.0f / 1024.0f), &sn, &cs);
        tw[m] = make_float2(cs, -sn);
    }
    DFT<8, -1>::run(v);
#pragma unroll
    for (int r = 0; r < 8; ++r) bA[tid * 8 + r] = v[r];
    __syncthreads();
    stockham_pass<8, -1>(bA, bB, tid, NS, 8, tw);
    __syncthreads();
    stockham_pass<8, -1>(bB, bA, tid, NS, 64, tw);
    __syncthreads();
#pragma unroll
    for (int b = 0; b < 2; ++b) {
        const int j = tid + 256 * b;
        float2 u[4];
#pragma unroll
        for (int r = 0; r < 4; ++r) u[r] = bA[j + 512 * r];
#pragma unroll
        for (int r = 1; r < 4; ++r) u[r] = cmul(u[r], tw[j * r]);
        DFT<4, -1>::run(u);
#pragma unroll
        for (int r = 0; r < 4; ++r) bB[j + 512 * r] = cmul(u[r], wv[b * 4 + r]);
    }
    __syncthreads();
    stockham_pass<8, 1>(bB, bA, tid, NS, 1, tw);
    __syncthreads();
    stockham_pass<8, 1>(bA, bB, tid, NS, 8, tw);
    __syncthreads();
    stockham_pass<8, 1>(bB, bA, tid, NS, 64, tw);
    __syncthreads();
    const float scale = 1.0f / ((float)NS * (float)ND);
#pragma unroll
    for (int b = 0; b < 2; ++b) {
        const int j = tid + 256 * b;
        float2 u[4];
#pragma unroll
        for (int r = 0; r < 4; ++r) u[r] = bA[j + 512 * r];
#pragma unroll
        for (int r = 1; r < 4; ++r) {
            float2 w = tw[j * r];
            w.y = -w.y;
            u[r] = cmul(u[r], w);
        }
        DFT<4, 1>::run(u);
#pragma unroll
        for (int r = 0; r < 4; ++r) col[j + 512 * r] = make_float2(u[r].x * scale, u[r].y * scale);
    }
}

__global__ __launch_bounds__(256) void k_stage3(const float2* __restrict__ spec, const __hip_bfloat16* __restrict__ x,
                                                __hip_bfloat16* __restrict__ hbuf,
                                                __hip_bfloat16* __restrict__ hn) {
    __shared__ __attribute__((aligned(16))) unsigned char smem[SMEM_FFT640];
    float2* tw = (float2*)smem;
    float2* bufA = tw + ND;
    float2* bufB = bufA + 4 * ND;
    const int tid = threadIdx.x;
    const int s0 = blockIdx.x * 8;
    for (int m = tid; m < ND; m += 256) {
        float sn, cs;
        sincospif((float)m * (1.0f / 320.0f), &sn, &cs);
        tw[m] = make_float2(cs, -sn);
    }
    for (int it = tid; it < NKF * 4; it += 256) {
        const int k = it >> 2, p = it & 3;
        float2 ya = spec[(size_t)k * NS + s0 + 2 * p];
        float2 yb = spec[(size_t)k * NS + s0 + 2 * p + 1];
        if (k == 0 || k == ND / 2) { ya.y = 0.f; yb.y = 0.f; }
        bufA[p * ND + k] = make_float2(ya.x - yb.y, ya.y + yb.x);
        if (k > 0 && k < ND / 2) bufA[p * ND + ND - k] = make_float2(ya.x + yb.y, yb.x - ya.y);
    }
    __syncthreads();
    for (int j = tid; j < 4 * (ND / 8); j += 256) {
        const int f = j / (ND / 8), jj = j % (ND / 8);
        stockham_pass<8, 1>(bufA + f * ND, bufB + f * ND, jj, ND, 1, tw);
    }
    __syncthreads();
    for (int j = tid; j < 4 * (ND / 8); j += 256) {
        const int f = j / (ND / 8), jj = j % (ND / 8);
        stockham_pass<8, 1>(bufB + f * ND, bufA + f * ND, jj, ND, 8, tw);
    }
    __syncthreads();
    for (int j = tid; j < 4 * (ND / 10); j += 256) {
        const int f = j / (ND / 10), jj = j % (ND / 10);
        stockham_pass<10, 1>(bufA + f * ND, bufB + f * ND, jj, ND, 64, tw);
    }
    __syncthreads();
    const int wave = tid >> 6, lane = tid & 63;
    for (int rr = 0; rr < 2; ++rr) {
        const int row = s0 + wave * 2 + rr;
        const float* src = (const float*)(bufB + wave * ND);
        float hv[10], u[10], u2[10];
#pragma unroll
        for (int i = 0; i < 10; ++i) {
            const int d = lane + 64 * i;
            const float y = bfr(src[d * 2 + rr]);
            hv[i] = bfr(__bfloat162float(x[(size_t)row * ND + d]) + y);
        }
        ln_row_regs(hv, u);
        ln_row_regs(u, u2);
#pragma unroll
        for (int i = 0; i < 10; ++i) {
            const int d = lane + 64 * i;
            hbuf[(size_t)row * ND + d] = __float2bfloat16(hv[i]);
            hn[(size_t)row * ND + d] = __float2bfloat16(u2[i]);
        }
    }
}

template <int BM, int BN, int WM, int WN, int EPI>
__global__ __launch_bounds__(256, 2) void k_gemm(const __hip_bfloat16* __restrict__ A,
                                                 const __hip_bfloat16* __restrict__ Bt, int M, int N, int K,
                                                 const __hip_bfloat16* __restrict__ bias,
                                                 const __hip_bfloat16* __restrict__ hres,
                                                 __hip_bfloat16* __restrict__ outp) {
    constexpr int WTM = BM / WM, WTN = BN / WN, MT = WTM / 16, NT = WTN / 16;
    constexpr int NCA = (BM * 8 + 255) / 256, NCB = (BN * 8 + 255) / 256;
    constexpr int ABYTES = BM * 128, BBYTES = BN * 128, SBYTES = ABYTES + BBYTES;
    __shared__ __attribute__((aligned(16))) unsigned char smb[2 * SBYTES];
    const int tid = threadIdx.x, w = tid >> 6, lane = tid & 63;
    const int wm = w / WN, wn = w % WN;
    const int mtiles = M / BM;
    const int mper = mtiles / 8;
    const int xcd = blockIdx.x % 8, idx = blockIdx.x / 8;
    const int m0 = (xcd * mper + idx % mper) * BM;
    const int n0 = (idx / mper) * BN;
    const int fr = lane & 15, fq = lane >> 4;

    f32x4_t acc[MT][NT];
#pragma unroll
    for (int i = 0; i < MT; ++i)
#pragma unroll
        for (int j = 0; j < NT; ++j) acc[i][j] = f32x4_t{0.f, 0.f, 0.f, 0.f};

    uint4 ra[NCA], rb[NCB];
#define GEMM_GLOAD(k0)                                                                        \
    _Pragma("unroll") for (int i = 0; i < NCA; ++i) {                                         \
        const int c = min(tid + i * 256, BM * 8 - 1);                                         \
        ra[i] = *(const uint4*)(A + (size_t)(m0 + (c >> 3)) * K + (k0) + (c & 7) * 8);        \
    }                                                                                         \
    _Pragma("unroll") for (int i = 0; i < NCB; ++i) {                                         \
        const int c = min(tid + i * 256, BN * 8 - 1);                                         \
        const int n = min(n0 + (c >> 3), N - 1);                                              \
        rb[i] = *(const uint4*)(Bt + (size_t)n * K + (k0) + (c & 7) * 8);                     \
    }
#define GEMM_SWRITE(buf)                                                                      \
    {                                                                                         \
        unsigned char* sa_ = smb + (buf) * SBYTES;                                            \
        unsigned char* sb_ = sa_ + ABYTES;                                                    \
        _Pragma("unroll") for (int i = 0; i < NCA; ++i) {                                     \
            const int c = min(tid + i * 256, BM * 8 - 1);                                     \
            const int row = c >> 3, c8 = c & 7;                                               \
            *(uint4*)(sa_ + row * 128 + ((c8 ^ (row & 7)) << 4)) = ra[i];                     \
        }                                                                                     \
        _Pragma("unroll") for (int i = 0; i < NCB; ++i) {                                     \
            const int c = min(tid + i * 256, BN * 8 - 1);                                     \
            const int row = c >> 3, c8 = c & 7;                                               \
            *(uint4*)(sb_ + row * 128 + ((c8 ^ (row & 7)) << 4)) = rb[i];                     \
        }                                                                                     \
    }
    const int nk = K / 64;
    GEMM_GLOAD(0)
    GEMM_SWRITE(0)
    __syncthreads();
    for (int kt = 0; kt < nk; ++kt) {
        const int knext = (kt + 1 < nk) ? (kt + 1) * 64 : kt * 64;
        GEMM_GLOAD(knext)
        {
            const unsigned char* sa = smb + (kt & 1) * SBYTES;
            const unsigned char* sb = sa + ABYTES;
#pragma unroll
            for (int kk = 0; kk < 2; ++kk) {
                bf16x8_t af[MT], bfv[NT];
#pragma unroll
                for (int i = 0; i < MT; ++i) {
                    const int row = wm * WTM + i * 16 + fr;
                    af[i] = *(const bf16x8_t*)(sa + row * 128 + (((kk * 4 + fq) ^ (row & 7)) << 4));
                }
#pragma unroll
                for (int j = 0; j < NT; ++j) {
                    const int row = wn * WTN + j * 16 + fr;
                    bfv[j] = *(const bf16x8_t*)(sb + row * 128 + (((kk * 4 + fq) ^ (row & 7)) << 4));
                }
#pragma unroll
                for (int i = 0; i < MT; ++i)
#pragma unroll
                    for (int j = 0; j < NT; ++j)
                        acc[i][j] = __builtin_amdgcn_mfma_f32_16x16x32_bf16(af[i], bfv[j], acc[i][j], 0, 0, 0);
            }
        }
        GEMM_SWRITE((kt + 1) & 1)
        __syncthreads();
    }
#undef GEMM_GLOAD
#undef GEMM_SWRITE

#pragma unroll
    for (int i = 0; i < MT; ++i)
#pragma unroll
        for (int j = 0; j < NT; ++j) {
            const int col = n0 + wn * WTN + j * 16 + fr;
            if (col < N) {
                const float bcol = __bfloat162float(bias[col]);
#pragma unroll
                for (int e = 0; e < 4; ++e) {
                    const int row = m0 + wm * WTM + i * 16 + fq * 4 + e;
                    const float accv = acc[i][j][e];
                    if (EPI == 1) {
                        const float s1 = bfr(accv);
                        const float s2 = bfr(s1 + bcol);
                        const float g =
                            0.5f * s2 * (1.0f + tanhf(0.7978845608028654f * (s2 + 0.044715f * s2 * s2 * s2)));
                        outp[(size_t)row * N + col] = __float2bfloat16(g);
                    } else {
                        const float e1 = bfr(accv);
                        const float e2 = bfr(e1 + bcol);
                        const float h = __bfloat162float(hres[(size_t)row * N + col]);
                        outp[(size_t)row * N + col] = __float2bfloat16(h + e2);
                    }
                }
            }
        }
}

extern "C" void kernel_launch(void* const* d_in, const int* in_sizes, int n_in, void* d_out, int out_size,
                              void* d_ws, size_t ws_size, hipStream_t stream) {
    const __hip_bfloat16* x = (const __hip_bfloat16*)d_in[0];
    const __hip_bfloat16* wr = (const __hip_bfloat16*)d_in[1];
    const __hip_bfloat16* wi = (const __hip_bfloat16*)d_in[2];
    const __hip_bfloat16* w1 = (const __hip_bfloat16*)d_in[4];
    const __hip_bfloat16* b1 = (const __hip_bfloat16*)d_in[5];
    const __hip_bfloat16* w2 = (const __hip_bfloat16*)d_in[6];
    const __hip_bfloat16* b2 = (const __hip_bfloat16*)d_in[7];
    __hip_bfloat16* out = (__hip_bfloat16*)d_out;

    char* ws = (char*)d_ws;
    auto align = [](size_t v) { return (v + 255) & ~(size_t)255; };
    size_t off = 0;
    float2* spec = (float2*)(ws + off);            off = align(off + (size_t)NKF * NS * 8);
    __hip_bfloat16* hn = (__hip_bfloat16*)(ws + off);   off = align(off + (size_t)NS * ND * 2);
    __hip_bfloat16* hb = (__hip_bfloat16*)(ws + off);   off = align(off + (size_t)NS * ND * 2);
    __hip_bfloat16* hid = (__hip_bfloat16*)(ws + off);  off = align(off + (size_t)NS * NF * 2);
    __hip_bfloat16* w1t = (__hip_bfloat16*)(ws + off);  off = align(off + (size_t)NF * ND * 2);
    __hip_bfloat16* w2t = (__hip_bfloat16*)(ws + off);  off = align(off + (size_t)ND * NF * 2);

    k_stage1<<<S1_FFT_BLOCKS + TR_W1_TILES + TR_W2_TILES, 256, 0, stream>>>(
        x, spec, (const unsigned short*)w1, (unsigned short*)w1t, (const unsigned short*)w2,
        (unsigned short*)w2t);
    k_stage2<<<NKF, 256, 0, stream>>>(spec, wr, wi);
    k_stage3<<<NS / 8, 256, 0, stream>>>(spec, x, hb, hn);
    k_gemm<128, 128, 2, 2, 1><<<(NS / 128) * ((NF + 127) / 128), 256, 0, stream>>>(hn, w1t, NS, NF, ND, b1,
                                                                                  nullptr, hid);
    k_gemm<64, 80, 4, 1, 2><<<(NS / 64) * (ND / 80), 256, 0, stream>>>(hid, w2t, NS, ND, NF, b2, hb, out);
}
```

```cpp
#include <hip/hip_runtime.h>
#include <hip/hip_bf16.h>
#include <stdint.h>


#define NS 2048
#define ND 640
#define NKF 321
#define NF 1728

typedef __attribute__((ext_vector_type(8))) short bf16x8_t;
typedef __attribute__((ext_vector_type(4))) float f32x4_t;
typedef float v2f __attribute__((ext_vector_type(2)));
typedef unsigned u32x4 __attribute__((ext_vector_type(4)));

__device__ const float TW640[1280] = {
1.0e+00f,-0.0e+00f,9.999518e-01f,-9.817319e-03f,9.9980724e-01f,-1.9633692e-02f,9.995663e-01f,-2.9448174e-02f,
9.99229e-01f,-3.9259817e-02f,9.9879545e-01f,-4.9067676e-02f,9.982656e-01f,-5.8870804e-02f,9.9763954e-01f,-6.866826e-02f,
9.969173e-01f,-7.84591e-02f,9.9609905e-01f,-8.824237e-02f,9.951847e-01f,-9.801714e-02f,9.941745e-01f,-1.0778246e-01f,
9.9306846e-01f,-1.17537394e-01f,9.918667e-01f,-1.2728101e-01f,9.9056935e-01f,-1.3701235e-01f,9.891765e-01f,-1.4673047e-01f,
9.8768836e-01f,-1.5643446e-01f,9.8610497e-01f,-1.6612338e-01f,9.8442656e-01f,-1.7579629e-01f,9.8265326e-01f,-1.8545224e-01f,
9.8078525e-01f,-1.9509032e-01f,9.7882277e-01f,-2.047096e-01f,9.767659e-01f,-2.1430916e-01f,9.7461486e-01f,-2.2388805e-01f,
9.723699e-01f,-2.3344536e-01f,9.7003126e-01f,-2.4298018e-01f,9.675991e-01f,-2.5249156e-01f,9.6507365e-01f,-2.6197863e-01f,
9.624552e-01f,-2.7144045e-01f,9.5974404e-01f,-2.808761e-01f,9.5694035e-01f,-2.9028466e-01f,9.540444e-01f,-2.9966527e-01f,
9.5105654e-01f,-3.09017e-01f,9.4797695e-01f,-3.1833893e-01f,9.4480604e-01f,-3.276302e-01f,9.4154406e-01f,-3.3688986e-01f,
9.3819135e-01f,-3.4611705e-01f,9.347482e-01f,-3.553109e-01f,9.312149e-01f,-3.644705e-01f,9.275919e-01f,-3.7359497e-01f,
9.238795e-01f,-3.8268343e-01f,9.200781e-01f,-3.9173502e-01f,9.1618794e-01f,-4.0074882e-01f,9.122095e-01f,-4.0972403e-01f,
9.0814316e-01f,-4.1865975e-01f,9.039893e-01f,-4.2755508e-01f,8.9974827e-01f,-4.3640924e-01f,8.9542055e-01f,-4.4522133e-01f,
8.910065e-01f,-4.539905e-01f,8.865066e-01f,-4.6271592e-01f,8.819213e-01f,-4.7139674e-01f,8.772509e-01f,-4.8003212e-01f,
8.72496e-01f,-4.8862123e-01f,8.67657e-01f,-4.9716327e-01f,8.627344e-01f,-5.056574e-01f,8.577286e-01f,-5.1410276e-01f,
8.5264015e-01f,-5.2249855e-01f,8.4746957e-01f,-5.3084403e-01f,8.422172e-01f,-5.391383e-01f,8.368837e-01f,-5.473807e-01f,
8.314696e-01f,-5.5557024e-01f,8.2597536e-01f,-5.637063e-01f,8.2040143e-01f,-5.7178795e-01f,8.1474847e-01f,-5.7981455e-01f,
8.09017e-01f,-5.8778524e-01f,8.032075e-01f,-5.956993e-01f,7.9732066e-01f,-6.035559e-01f,7.913569e-01f,-6.113544e-01f,
7.8531694e-01f,-6.1909395e-01f,7.7920127e-01f,-6.2677383e-01f,7.7301043e-01f,-6.343933e-01f,7.6674515e-01f,-6.419516e-01f,
7.6040596e-01f,-6.4944804e-01f,7.539935e-01f,-6.568819e-01f,7.4750835e-01f,-6.6425246e-01f,7.409511e-01f,-6.71559e-01f,
7.343225e-01f,-6.7880076e-01f,7.276231e-01f,-6.859771e-01f,7.2085357e-01f,-6.9308734e-01f,7.140146e-01f,-7.001308e-01f,
7.0710677e-01f,-7.0710677e-01f,7.001308e-01f,-7.140146e-01f,6.9308734e-01f,-7.2085357e-01f,6.859771e-01f,-7.276231e-01f,
6.7880076e-01f,-7.343225e-01f,6.71559e-01f,-7.409511e-01f,6.6425246e-01f,-7.4750835e-01f,6.568819e-01f,-7.539935e-01f,
6.4944804e-01f,-7.6040596e-01f,6.419516e-01f,-7.6674515e-01f,6.343933e-01f,-7.7301043e-01f,6.2677383e-01f,-7.7920127e-01f,
6.1909395e-01f,-7.8531694e-01f,6.113544e-01f,-7.913569e-01f,6.035559e-01f,-7.9732066e-01f,5.956993e-01f,-8.032075e-01f,
5.8778524e-01f,-8.09017e-01f,5.7981455e-01f,-8.1474847e-01f,5.7178795e-01f,-8.2040143e-01f,5.637063e-01f,-8.2597536e-01f,
5.5557024e-01f,-8.314696e-01f,5.473807e-01f,-8.368837e-01f,5.391383e-01f,-8.422172e-01f,5.3084403e-01f,-8.4746957e-01f,
5.2249855e-01f,-8.5264015e-01f,5.1410276e-01f,-8.577286e-01f,5.056574e-01f,-8.627344e-01f,4.9716327e-01f,-8.67657e-01f,
4.8862123e-01f,-8.72496e-01f,4.8003212e-01f,-8.772509e-01f,4.7139674e-01f,-8.819213e-01f,4.6271592e-01f,-8.865066e-01f,
4.539905e-01f,-8.910065e-01f,4.4522133e-01f,-8.9542055e-01f,4.3640924e-01f,-8.9974827e-01f,4.2755508e-01f,-9.039893e-01f,
4.1865975e-01f,-9.0814316e-01f,4.0972403e-01f,-9.122095e-01f,4.0074882e-01f,-9.1618794e-01f,3.9173502e-01f,-9.200781e-01f,
3.8268343e-01f,-9.238795e-01f,3.7359497e-01f,-9.275919e-01f,3.644705e-01f,-9.312149e-01f,3.553109e-01f,-9.347482e-01f,
3.4611705e-01f,-9.3819135e-01f,3.3688986e-01f,-9.4154406e-01f,3.276302e-01f,-9.4480604e-01f,3.1833893e-01f,-9.4797695e-01f,
3.09017e-01f,-9.5105654e-01f,2.9966527e-01f,-9.540444e-01f,2.9028466e-01f,-9.5694035e-01f,2.808761e-01f,-9.5974404e-01f,
2.7144045e-01f,-9.624552e-01f,2.6197863e-01f,-9.6507365e-01f,2.5249156e-01f,-9.675991e-01f,2.4298018e-01f,-9.7003126e-01f,
2.3344536e-01f,-9.723699e-01f,2.2388805e-01f,-9.7461486e-01f,2.1430916e-01f,-9.767659e-01f,2.047096e-01f,-9.7882277e-01f,
1.9509032e-01f,-9.8078525e-01f,1.8545224e-01f,-9.8265326e-01f,1.7579629e-01f,-9.8442656e-01f,1.6612338e-01f,-9.8610497e-01f,
1.5643446e-01f,-9.8768836e-01f,1.4673047e-01f,-9.891765e-01f,1.3701235e-01f,-9.9056935e-01f,1.2728101e-01f,-9.918667e-01f,
1.17537394e-01f,-9.9306846e-01f,1.0778246e-01f,-9.941745e-01f,9.801714e-02f,-9.951847e-01f,8.824237e-02f,-9.9609905e-01f,
7.84591e-02f,-9.969173e-01f,6.866826e-02f,-9.9763954e-01f,5.8870804e-02f,-9.982656e-01f,4.9067676e-02f,-9.9879545e-01f,
3.9259817e-02f,-9.99229e-01f,2.9448174e-02f,-9.995663e-01f,1.9633692e-02f,-9.9980724e-01f,9.817319e-03f,-9.999518e-01f,
6.123234e-17f,-1.0e+00f,-9.817319e-03f,-9.999518e-01f,-1.9633692e-02f,-9.9980724e-01f,-2.9448174e-02f,-9.995663e-01f,
-3.9259817e-02f,-9.99229e-01f,-4.9067676e-02f,-9.9879545e-01f,-5.8870804e-02f,-9.982656e-01f,-6.866826e-02f,-9.9763954e-01f,
-7.84591e-02f,-9.969173e-01f,-8.824237e-02f,-9.9609905e-01f,-9.801714e-02f,-9.951847e-01f,-1.0778246e-01f,-9.941745e-01f,
-1.17537394e-01f,-9.9306846e-01f,-1.2728101e-01f,-9.918667e-01f,-1.3701235e-01f,-9.9056935e-01f,-1.4673047e-01f,-9.891765e-01f,
-1.5643446e-01f,-9.8768836e-01f,-1.6612338e-01f,-9.8610497e-01f,-1.7579629e-01f,-9.8442656e-01f,-1.8545224e-01f,-9.8265326e-01f,
-1.9509032e-01f,-9.8078525e-01f,-2.047096e-01f,-9.7882277e-01f,-2.1430916e-01f,-9.767659e-01f,-2.2388805e-01f,-9.7461486e-01f,
-2.3344536e-01f,-9.723699e-01f,-2.4298018e-01f,-9.7003126e-01f,-2.5249156e-01f,-9.675991e-01f,-2.6197863e-01f,-9.6507365e-01f,
-2.7144045e-01f,-9.624552e-01f,-2.808761e-01f,-9.5974404e-01f,-2.9028466e-01f,-9.5694035e-01f,-2.9966527e-01f,-9.540444e-01f,
-3.09017e-01f,-9.5105654e-01f,-3.1833893e-01f,-9.4797695e-01f,-3.276302e-01f,-9.4480604e-01f,-3.3688986e-01f,-9.4154406e-01f,
-3.4611705e-01f,-9.3819135e-01f,-3.553109e-01f,-9.347482e-01f,-3.644705e-01f,-9.312149e-01f,-3.7359497e-01f,-9.275919e-01f,
-3.8268343e-01f,-9.238795e-01f,-3.9173502e-01f,-9.200781e-01f,-4.0074882e-01f,-9.1618794e-01f,-4.0972403e-01f,-9.122095e-01f,
-4.1865975e-01f,-9.0814316e-01f,-4.2755508e-01f,-9.039893e-01f,-4.3640924e-01f,-8.9974827e-01f,-4.4522133e-01f,-8.9542055e-01f,
-4.539905e-01f,-8.910065e-01f,-4.6271592e-01f,-8.865066e-01f,-4.7139674e-01f,-8.819213e-01f,-4.8003212e-01f,-8.772509e-01f,
-4.8862123e-01f,-8.72496e-01f,-4.9716327e-01f,-8.67657e-01f,-5.056574e-01f,-8.627344e-01f,-5.1410276e-01f,-8.577286e-01f,
-5.2249855e-01f,-8.5264015e-01f,-5.3084403e-01f,-8.4746957e-01f,-5.391383e-01f,-8.422172e-01f,-5.473807e-01f,-8.368837e-01f,
-5.5557024e-01f,-8.314696e-01f,-5.637063e-01f,-8.2597536e-01f,-5.7178795e-01f,-8.2040143e-01f,-5.7981455e-01f,-8.1474847e-01f,
-5.8778524e-01f,-8.09017e-01f,-5.956993e-01f,-8.032075e-01f,-6.035559e-01f,-7.9732066e-01f,-6.113544e-01f,-7.913569e-01f,
-6.1909395e-01f,-7.8531694e-01f,-6.2677383e-01f,-7.7920127e-01f,-6.343933e-01f,-7.7301043e-01f,-6.419516e-01f,-7.6674515e-01f,
-6.4944804e-01f,-7.6040596e-01f,-6.568819e-01f,-7.539935e-01f,-6.6425246e-01f,-7.4750835e-01f,-6.71559e-01f,-7.409511e-01f,
-6.7880076e-01f,-7.343225e-01f,-6.859771e-01f,-7.276231e-01f,-6.9308734e-01f,-7.2085357e-01f,-7.001308e-01f,-7.140146e-01f,
-7.0710677e-01f,-7.0710677e-01f,-7.140146e-01f,-7.001308e-01f,-7.2085357e-01f,-6.9308734e-01f,-7.276231e-01f,-6.859771e-01f,
-7.343225e-01f,-6.7880076e-01f,-7.409511e-01f,-6.71559e-01f,-7.4750835e-01f,-6.6425246e-01f,-7.539935e-01f,-6.568819e-01f,
-7.6040596e-01f,-6.4944804e-01f,-7.6674515e-01f,-6.419516e-01f,-7.7301043e-01f,-6.343933e-01f,-7.7920127e-01f,-6.2677383e-01f,
-7.8531694e-01f,-6.1909395e-01f,-7.913569e-01f,-6.113544e-01f,-7.9732066e-01f,-6.035559e-01f,-8.032075e-01f,-5.956993e-01f,
-8.09017e-01f,-5.8778524e-01f,-8.1474847e-01f,-5.7981455e-01f,-8.2040143e-01f,-5.7178795e-01f,-8.2597536e-01f,-5.637063e-01f,
-8.314696e-01f,-5.5557024e-01f,-8.368837e-01f,-5.473807e-01f,-8.422172e-01f,-5.391383e-01f,-8.4746957e-01f,-5.3084403e-01f,
-8.5264015e-01f,-5.2249855e-01f,-8.577286e-01f,-5.1410276e-01f,-8.627344e-01f,-5.056574e-01f,-8.67657e-01f,-4.9716327e-01f,
-8.72496e-01f,-4.8862123e-01f,-8.772509e-01f,-4.8003212e-01f,-8.819213e-01f,-4.7139674e-01f,-8.865066e-01f,-4.6271592e-01f,
-8.910065e-01f,-4.539905e-01f,-8.9542055e-01f,-4.4522133e-01f,-8.9974827e-01f,-4.3640924e-01f,-9.039893e-01f,-4.2755508e-01f,
-9.0814316e-01f,-4.1865975e-01f,-9.122095e-01f,-4.0972403e-01f,-9.1618794e-01f,-4.0074882e-01f,-9.200781e-01f,-3.9173502e-01f,
-9.238795e-01f,-3.8268343e-01f,-9.275919e-01f,-3.7359497e-01f,-9.312149e-01f,-3.644705e-01f,-9.347482e-01f,-3.553109e-01f,
-9.3819135e-01f,-3.4611705e-01f,-9.4154406e-01f,-3.3688986e-01f,-9.4480604e-01f,-3.276302e-01f,-9.4797695e-01f,-3.1833893e-01f,
-9.5105654e-01f,-3.09017e-01f,-9.540444e-01f,-2.9966527e-01f,-9.5694035e-01f,-2.9028466e-01f,-9.5974404e-01f,-2.808761e-01f,
-9.624552e-01f,-2.7144045e-01f,-9.6507365e-01f,-2.6197863e-01f,-9.675991e-01f,-2.5249156e-01f,-9.7003126e-01f,-2.4298018e-01f,
-9.723699e-01f,-2.3344536e-01f,-9.7461486e-01f,-2.2388805e-01f,-9.767659e-01f,-2.1430916e-01f,-9.7882277e-01f,-2.047096e-01f,
-9.8078525e-01f,-1.9509032e-01f,-9.8265326e-01f,-1.8545224e-01f,-9.8442656e-01f,-1.7579629e-01f,-9.8610497e-01f,-1.6612338e-01f,
-9.8768836e-01f,-1.5643446e-01f,-9.891765e-01f,-1.4673047e-01f,-9.9056935e-01f,-1.3701235e-01f,-9.918667e-01f,-1.2728101e-01f,
-9.9306846e-01f,-1.17537394e-01f,-9.941745e-01f,-1.0778246e-01f,-9.951847e-01f,-9.801714e-02f,-9.9609905e-01f,-8.824237e-02f,
-9.969173e-01f,-7.84591e-02f,-9.9763954e-01f,-6.866826e-02f,-9.982656e-01f,-5.8870804e-02f,-9.9879545e-01f,-4.9067676e-02f,
-9.99229e-01f,-3.9259817e-02f,-9.995663e-01f,-2.9448174e-02f,-9.9980724e-01f,-1.9633692e-02f,-9.999518e-01f,-9.817319e-03f,
-1.0e+00f,-1.2246469e-16f,-9.999518e-01f,9.817319e-03f,-9.9980724e-01f,1.9633692e-02f,-9.995663e-01f,2.9448174e-02f,
-9.99229e-01f,3.9259817e-02f,-9.9879545e-01f,4.9067676e-02f,-9.982656e-01f,5.8870804e-02f,-9.9763954e-01f,6.866826e-02f,
-9.969173e-01f,7.84591e-02f,-9.9609905e-01f,8.824237e-02f,-9.951847e-01f,9.801714e-02f,-9.941745e-01f,1.0778246e-01f,
-9.9306846e-01f,1.17537394e-01f,-9.918667e-01f,1.2728101e-01f,-9.9056935e-01f,1.3701235e-01f,-9.891765e-01f,1.4673047e-01f,
-9.8768836e-01f,1.5643446e-01f,-9.8610497e-01f,1.6612338e-01f,-9.8442656e-01f,1.7579629e-01f,-9.8265326e-01f,1.8545224e-01f,
-9.8078525e-01f,1.9509032e-01f,-9.7882277e-01f,2.047096e-01f,-9.767659e-01f,2.1430916e-01f,-9.7461486e-01f,2.2388805e-01f,
-9.723699e-01f,2.3344536e-01f,-9.7003126e-01f,2.4298018e-01f,-9.675991e-01f,2.5249156e-01f,-9.6507365e-01f,2.6197863e-01f,
-9.624552e-01f,2.7144045e-01f,-9.5974404e-01f,2.808761e-01f,-9.5694035e-01f,2.9028466e-01f,-9.540444e-01f,2.9966527e-01f,
-9.5105654e-01f,3.09017e-01f,-9.4797695e-01f,3.1833893e-01f,-9.4480604e-01f,3.276302e-01f,-9.4154406e-01f,3.3688986e-01f,
-9.3819135e-01f,3.4611705e-01f,-9.347482e-01f,3.553109e-01f,-9.312149e-01f,3.644705e-01f,-9.275919e-01f,3.7359497e-01f,
-9.238795e-01f,3.8268343e-01f,-9.200781e-01f,3.9173502e-01f,-9.1618794e-01f,4.0074882e-01f,-9.122095e-01f,4.0972403e-01f,
-9.0814316e-01f,4.1865975e-01f,-9.039893e-01f,4.2755508e-01f,-8.9974827e-01f,4.3640924e-01f,-8.9542055e-01f,4.4522133e-01f,
-8.910065e-01f,4.539905e-01f,-8.865066e-01f,4.6271592e-01f,-8.819213e-01f,4.7139674e-01f,-8.772509e-01f,4.8003212e-01f,
-8.72496e-01f,4.8862123e-01f,-8.67657e-01f,4.9716327e-01f,-8.627344e-01f,5.056574e-01f,-8.577286e-01f,5.1410276e-01f,
-8.5264015e-01f,5.2249855e-01f,-8.4746957e-01f,5.3084403e-01f,-8.422172e-01f,5.391383e-01f,-8.368837e-01f,5.473807e-01f,
-8.314696e-01f,5.5557024e-01f,-8.2597536e-01f,5.637063e-01f,-8.2040143e-01f,5.7178795e-01f,-8.1474847e-01f,5.7981455e-01f,
-8.09017e-01f,5.8778524e-01f,-8.032075e-01f,5.956993e-01f,-7.9732066e-01f,6.035559e-01f,-7.913569e-01f,6.113544e-01f,
-7.8531694e-01f,6.1909395e-01f,-7.7920127e-01f,6.2677383e-01f,-7.7301043e-01f,6.343933e-01f,-7.6674515e-01f,6.419516e-01f,
-7.6040596e-01f,6.4944804e-01f,-7.539935e-01f,6.568819e-01f,-7.4750835e-01f,6.6425246e-01f,-7.409511e-01f,6.71559e-01f,
-7.343225e-01f,6.7880076e-01f,-7.276231e-01f,6.859771e-01f,-7.2085357e-01f,6.9308734e-01f,-7.140146e-01f,7.001308e-01f,
-7.0710677e-01f,7.0710677e-01f,-7.001308e-01f,7.140146e-01f,-6.9308734e-01f,7.2085357e-01f,-6.859771e-01f,7.276231e-01f,
-6.7880076e-01f,7.343225e-01f,-6.71559e-01f,7.409511e-01f,-6.6425246e-01f,7.4750835e-01f,-6.568819e-01f,7.539935e-01f,
-6.4944804e-01f,7.6040596e-01f,-6.419516e-01f,7.6674515e-01f,-6.343933e-01f,7.7301043e-01f,-6.2677383e-01f,7.7920127e-01f,
-6.1909395e-01f,7.8531694e-01f,-6.113544e-01f,7.913569e-01f,-6.035559e-01f,7.9732066e-01f,-5.956993e-01f,8.032075e-01f,
-5.8778524e-01f,8.09017e-01f,-5.7981455e-01f,8.1474847e-01f,-5.7178795e-01f,8.2040143e-01f,-5.637063e-01f,8.2597536e-01f,
-5.5557024e-01f,8.314696e-01f,-5.473807e-01f,8.368837e-01f,-5.391383e-01f,8.422172e-01f,-5.3084403e-01f,8.4746957e-01f,
-5.2249855e-01f,8.5264015e-01f,-5.1410276e-01f,8.577286e-01f,-5.056574e-01f,8.627344e-01f,-4.9716327e-01f,8.67657e-01f,
-4.8862123e-01f,8.72496e-01f,-4.8003212e-01f,8.772509e-01f,-4.7139674e-01f,8.819213e-01f,-4.6271592e-01f,8.865066e-01f,
-4.539905e-01f,8.910065e-01f,-4.4522133e-01f,8.9542055e-01f,-4.3640924e-01f,8.9974827e-01f,-4.2755508e-01f,9.039893e-01f,
-4.1865975e-01f,9.0814316e-01f,-4.0972403e-01f,9.122095e-01f,-4.0074882e-01f,9.1618794e-01f,-3.9173502e-01f,9.200781e-01f,
-3.8268343e-01f,9.238795e-01f,-3.7359497e-01f,9.275919e-01f,-3.644705e-01f,9.312149e-01f,-3.553109e-01f,9.347482e-01f,
-3.4611705e-01f,9.3819135e-01f,-3.3688986e-01f,9.4154406e-01f,-3.276302e-01f,9.4480604e-01f,-3.1833893e-01f,9.4797695e-01f,
-3.09017e-01f,9.5105654e-01f,-2.9966527e-01f,9.540444e-01f,-2.9028466e-01f,9.5694035e-01f,-2.808761e-01f,9.5974404e-01f,
-2.7144045e-01f,9.624552e-01f,-2.6197863e-01f,9.6507365e-01f,-2.5249156e-01f,9.675991e-01f,-2.4298018e-01f,9.7003126e-01f,
-2.3344536e-01f,9.723699e-01f,-2.2388805e-01f,9.7461486e-01f,-2.1430916e-01f,9.767659e-01f,-2.047096e-01f,9.7882277e-01f,
-1.9509032e-01f,9.8078525e-01f,-1.8545224e-01f,9.8265326e-01f,-1.7579629e-01f,9.8442656e-01f,-1.6612338e-01f,9.8610497e-01f,
-1.5643446e-01f,9.8768836e-01f,-1.4673047e-01f,9.891765e-01f,-1.3701235e-01f,9.9056935e-01f,-1.2728101e-01f,9.918667e-01f,
-1.17537394e-01f,9.9306846e-01f,-1.0778246e-01f,9.941745e-01f,-9.801714e-02f,9.951847e-01f,-8.824237e-02f,9.9609905e-01f,
-7.84591e-02f,9.969173e-01f,-6.866826e-02f,9.9763954e-01f,-5.8870804e-02f,9.982656e-01f,-4.9067676e-02f,9.9879545e-01f,
-3.9259817e-02f,9.99229e-01f,-2.9448174e-02f,9.995663e-01f,-1.9633692e-02f,9.9980724e-01f,-9.817319e-03f,9.999518e-01f,
-1.8369701e-16f,1.0e+00f,9.817319e-03f,9.999518e-01f,1.9633692e-02f,9.9980724e-01f,2.9448174e-02f,9.995663e-01f,
3.9259817e-02f,9.99229e-01f,4.9067676e-02f,9.9879545e-01f,5.8870804e-02f,9.982656e-01f,6.866826e-02f,9.9763954e-01f,
7.84591e-02f,9.969173e-01f,8.824237e-02f,9.9609905e-01f,9.801714e-02f,9.951847e-01f,1.0778246e-01f,9.941745e-01f,
1.17537394e-01f,9.9306846e-01f,1.2728101e-01f,9.918667e-01f,1.3701235e-01f,9.9056935e-01f,1.4673047e-01f,9.891765e-01f,
1.5643446e-01f,9.8768836e-01f,1.6612338e-01f,9.8610497e-01f,1.7579629e-01f,9.8442656e-01f,1.8545224e-01f,9.8265326e-01f,
1.9509032e-01f,9.8078525e-01f,2.047096e-01f,9.7882277e-01f,2.1430916e-01f,9.767659e-01f,2.2388805e-01f,9.7461486e-01f,
2.3344536e-01f,9.723699e-01f,2.4298018e-01f,9.7003126e-01f,2.5249156e-01f,9.675991e-01f,2.6197863e-01f,9.6507365e-01f,
2.7144045e-01f,9.624552e-01f,2.808761e-01f,9.5974404e-01f,2.9028466e-01f,9.5694035e-01f,2.9966527e-01f,9.540444e-01f,
3.09017e-01f,9.5105654e-01f,3.1833893e-01f,9.4797695e-01f,3.276302e-01f,9.4480604e-01f,3.3688986e-01f,9.4154406e-01f,
3.4611705e-01f,9.3819135e-01f,3.553109e-01f,9.347482e-01f,3.644705e-01f,9.312149e-01f,3.7359497e-01f,9.275919e-01f,
3.8268343e-01f,9.238795e-01f,3.9173502e-01f,9.200781e-01f,4.0074882e-01f,9.1618794e-01f,4.0972403e-01f,9.122095e-01f,
4.1865975e-01f,9.0814316e-01f,4.2755508e-01f,9.039893e-01f,4.3640924e-01f,8.9974827e-01f,4.4522133e-01f,8.9542055e-01f,
4.539905e-01f,8.910065e-01f,4.6271592e-01f,8.865066e-01f,4.7139674e-01f,8.819213e-01f,4.8003212e-01f,8.772509e-01f,
4.8862123e-01f,8.72496e-01f,4.9716327e-01f,8.67657e-01f,5.056574e-01f,8.627344e-01f,5.1410276e-01f,8.577286e-01f,
5.2249855e-01f,8.5264015e-01f,5.3084403e-01f,8.4746957e-01f,5.391383e-01f,8.422172e-01f,5.473807e-01f,8.368837e-01f,
5.5557024e-01f,8.314696e-01f,5.637063e-01f,8.2597536e-01f,5.7178795e-01f,8.2040143e-01f,5.7981455e-01f,8.1474847e-01f,
5.8778524e-01f,8.09017e-01f,5.956993e-01f,8.032075e-01f,6.035559e-01f,7.9732066e-01f,6.113544e-01f,7.913569e-01f,
6.1909395e-01f,7.8531694e-01f,6.2677383e-01f,7.7920127e-01f,6.343933e-01f,7.7301043e-01f,6.419516e-01f,7.6674515e-01f,
6.4944804e-01f,7.6040596e-01f,6.568819e-01f,7.539935e-01f,6.6425246e-01f,7.4750835e-01f,6.71559e-01f,7.409511e-01f,
6.7880076e-01f,7.343225e-01f,6.859771e-01f,7.276231e-01f,6.9308734e-01f,7.2085357e-01f,7.001308e-01f,7.140146e-01f,
7.0710677e-01f,7.0710677e-01f,7.140146e-01f,7.001308e-01f,7.2085357e-01f,6.9308734e-01f,7.276231e-01f,6.859771e-01f,
7.343225e-01f,6.7880076e-01f,7.409511e-01f,6.71559e-01f,7.4750835e-01f,6.6425246e-01f,7.539935e-01f,6.568819e-01f,
7.6040596e-01f,6.4944804e-01f,7.6674515e-01f,6.419516e-01f,7.7301043e-01f,6.343933e-01f,7.7920127e-01f,6.2677383e-01f,
7.8531694e-01f,6.1909395e-01f,7.913569e-01f,6.113544e-01f,7.9732066e-01f,6.035559e-01f,8.032075e-01f,5.956993e-01f,
8.09017e-01f,5.8778524e-01f,8.1474847e-01f,5.7981455e-01f,8.2040143e-01f,5.7178795e-01f,8.2597536e-01f,5.637063e-01f,
8.314696e-01f,5.5557024e-01f,8.368837e-01f,5.473807e-01f,8.422172e-01f,5.391383e-01f,8.4746957e-01f,5.3084403e-01f,
8.5264015e-01f,5.2249855e-01f,8.577286e-01f,5.1410276e-01f,8.627344e-01f,5.056574e-01f,8.67657e-01f,4.9716327e-01f,
8.72496e-01f,4.8862123e-01f,8.772509e-01f,4.8003212e-01f,8.819213e-01f,4.7139674e-01f,8.865066e-01f,4.6271592e-01f,
8.910065e-01f,4.539905e-01f,8.9542055e-01f,4.4522133e-01f,8.9974827e-01f,4.3640924e-01f,9.039893e-01f,4.2755508e-01f,
9.0814316e-01f,4.1865975e-01f,9.122095e-01f,4.0972403e-01f,9.1618794e-01f,4.0074882e-01f,9.200781e-01f,3.9173502e-01f,
9.238795e-01f,3.8268343e-01f,9.275919e-01f,3.7359497e-01f,9.312149e-01f,3.644705e-01f,9.347482e-01f,3.553109e-01f,
9.3819135e-01f,3.4611705e-01f,9.4154406e-01f,3.3688986e-01f,9.4480604e-01f,3.276302e-01f,9.4797695e-01f,3.1833893e-01f,
9.5105654e-01f,3.09017e-01f,9.540444e-01f,2.9966527e-01f,9.5694035e-01f,2.9028466e-01f,9.5974404e-01f,2.808761e-01f,
9.624552e-01f,2.7144045e-01f,9.6507365e-01f,2.6197863e-01f,9.675991e-01f,2.5249156e-01f,9.7003126e-01f,2.4298018e-01f,
9.723699e-01f,2.3344536e-01f,9.7461486e-01f,2.2388805e-01f,9.767659e-01f,2.1430916e-01f,9.7882277e-01f,2.047096e-01f,
9.8078525e-01f,1.9509032e-01f,9.8265326e-01f,1.8545224e-01f,9.8442656e-01f,1.7579629e-01f,9.8610497e-01f,1.6612338e-01f,
9.8768836e-01f,1.5643446e-01f,9.891765e-01f,1.4673047e-01f,9.9056935e-01f,1.3701235e-01f,9.918667e-01f,1.2728101e-01f,
9.9306846e-01f,1.17537394e-01f,9.941745e-01f,1.0778246e-01f,9.951847e-01f,9.801714e-02f,9.9609905e-01f,8.824237e-02f,
9.969173e-01f,7.84591e-02f,9.9763954e-01f,6.866826e-02f,9.982656e-01f,5.8870804e-02f,9.9879545e-01f,4.9067676e-02f,
9.99229e-01f,3.9259817e-02f,9.995663e-01f,2.9448174e-02f,9.9980724e-01f,1.9633692e-02f,9.999518e-01f,9.817319e-03f,
};
__device__ const float TW2048[4096] = {
1.0e+00f,-0.0e+00f,9.999953e-01f,-3.0679568e-03f,9.9998116e-01f,-6.1358847e-03f,9.999576e-01f,-9.203754e-03f,
9.999247e-01f,-1.2271538e-02f,9.9988234e-01f,-1.5339206e-02f,9.998306e-01f,-1.840673e-02f,9.997694e-01f,-2.147408e-02f,
9.996988e-01f,-2.4541229e-02f,9.996188e-01f,-2.7608145e-02f,9.995294e-01f,-3.0674804e-02f,9.994306e-01f,-3.3741172e-02f,
9.9932235e-01f,-3.6807224e-02f,9.9920475e-01f,-3.9872926e-02f,9.9907774e-01f,-4.293826e-02f,9.989413e-01f,-4.600318e-02f,
9.9879545e-01f,-4.9067676e-02f,9.9864024e-01f,-5.2131705e-02f,9.9847555e-01f,-5.5195246e-02f,9.9830157e-01f,-5.8258265e-02f,
9.981181e-01f,-6.1320737e-02f,9.979253e-01f,-6.438263e-02f,9.9772304e-01f,-6.744392e-02f,9.9751145e-01f,-7.0504576e-02f,
9.9729043e-01f,-7.356457e-02f,9.9706006e-01f,-7.6623864e-02f,9.968203e-01f,-7.968244e-02f,9.965711e-01f,-8.274026e-02f,
9.963126e-01f,-8.579731e-02f,9.960447e-01f,-8.885355e-02f,9.957674e-01f,-9.1908954e-02f,9.954808e-01f,-9.49635e-02f,
9.951847e-01f,-9.801714e-02f,9.948793e-01f,-1.0106986e-01f,9.945646e-01f,-1.0412163e-01f,9.9424046e-01f,-1.0717242e-01f,
9.93907e-01f,-1.10222206e-01f,9.935641e-01f,-1.1327095e-01f,9.932119e-01f,-1.1631863e-01f,9.928504e-01f,-1.19365215e-01f,
9.9247956e-01f,-1.2241068e-01f,9.920993e-01f,-1.2545498e-01f,9.9170977e-01f,-1.284981e-01f,9.9131083e-01f,-1.3154003e-01f,
9.9090266e-01f,-1.345807e-01f,9.904851e-01f,-1.3762012e-01f,9.900582e-01f,-1.4065824e-01f,9.89622e-01f,-1.4369503e-01f,
9.891765e-01f,-1.4673047e-01f,9.8872167e-01f,-1.4976454e-01f,9.882576e-01f,-1.5279719e-01f,9.8778415e-01f,-1.558284e-01f,
9.873014e-01f,-1.5885815e-01f,9.868094e-01f,-1.618864e-01f,9.863081e-01f,-1.6491312e-01f,9.857975e-01f,-1.6793829e-01f,
9.8527765e-01f,-1.7096189e-01f,9.847485e-01f,-1.7398387e-01f,9.842101e-01f,-1.7700422e-01f,9.836624e-01f,-1.800229e-01f,
9.831055e-01f,-1.8303989e-01f,9.825393e-01f,-1.8605515e-01f,9.819639e-01f,-1.8906866e-01f,9.813792e-01f,-1.920804e-01f,
9.8078525e-01f,-1.9509032e-01f,9.801821e-01f,-1.980984e-01f,9.795698e-01f,-2.0110464e-01f,9.789482e-01f,-2.0410897e-01f,
9.783174e-01f,-2.0711137e-01f,9.7767735e-01f,-2.1011184e-01f,9.7702813e-01f,-2.1311031e-01f,9.7636974e-01f,-2.161068e-01f,
9.757021e-01f,-2.1910124e-01f,9.7502536e-01f,-2.2209363e-01f,9.7433937e-01f,-2.2508392e-01f,9.7364426e-01f,-2.2807208e-01f,
9.7293997e-01f,-2.310581e-01f,9.722265e-01f,-2.3404196e-01f,9.715039e-01f,-2.370236e-01f,9.7077215e-01f,-2.4000302e-01f,
9.7003126e-01f,-2.4298018e-01f,9.6928126e-01f,-2.4595505e-01f,9.685221e-01f,-2.4892761e-01f,9.677538e-01f,-2.518978e-01f,
9.6697646e-01f,-2.5486565e-01f,9.6619e-01f,-2.578311e-01f,9.6539444e-01f,-2.607941e-01f,9.645898e-01f,-2.6375467e-01f,
9.6377605e-01f,-2.6671275e-01f,9.6295327e-01f,-2.696683e-01f,9.621214e-01f,-2.7262136e-01f,9.6128047e-01f,-2.7557182e-01f,
9.604305e-01f,-2.785197e-01f,9.5957154e-01f,-2.8146493e-01f,9.5870346e-01f,-2.8440753e-01f,9.5782644e-01f,-2.8734747e-01f,
9.5694035e-01f,-2.9028466e-01f,9.5604527e-01f,-2.9321915e-01f,9.551412e-01f,-2.961509e-01f,9.542281e-01f,-2.9907984e-01f,
9.53306e-01f,-3.0200595e-01f,9.52375e-01f,-3.0492923e-01f,9.51435e-01f,-3.0784965e-01f,9.5048606e-01f,-3.1076714e-01f,
9.4952816e-01f,-3.1368175e-01f,9.485614e-01f,-3.1659338e-01f,9.475856e-01f,-3.1950203e-01f,9.466009e-01f,-3.224077e-01f,
9.456073e-01f,-3.253103e-01f,9.446048e-01f,-3.2820985e-01f,9.4359344e-01f,-3.311063e-01f,9.425732e-01f,-3.3399966e-01f,
9.4154406e-01f,-3.3688986e-01f,9.4050604e-01f,-3.3977687e-01f,9.394592e-01f,-3.4266073e-01f,9.3840355e-01f,-3.4554133e-01f,
9.37339e-01f,-3.4841868e-01f,9.3626565e-01f,-3.5129276e-01f,9.351835e-01f,-3.5416353e-01f,9.340925e-01f,-3.5703096e-01f,
9.329928e-01f,-3.5989505e-01f,9.318843e-01f,-3.6275572e-01f,9.3076694e-01f,-3.6561298e-01f,9.296409e-01f,-3.6846682e-01f,
9.285061e-01f,-3.713172e-01f,9.273625e-01f,-3.7416407e-01f,9.262102e-01f,-3.7700742e-01f,9.2504925e-01f,-3.798472e-01f,
9.238795e-01f,-3.8268343e-01f,9.227011e-01f,-3.8551605e-01f,9.2151403e-01f,-3.8834503e-01f,9.203183e-01f,-3.9117038e-01f,
9.191139e-01f,-3.9399204e-01f,9.179008e-01f,-3.9681e-01f,9.166791e-01f,-3.996242e-01f,9.154487e-01f,-4.0243465e-01f,
9.142098e-01f,-4.052413e-01f,9.129622e-01f,-4.0804416e-01f,9.1170603e-01f,-4.1084316e-01f,9.104413e-01f,-4.1363832e-01f,
9.09168e-01f,-4.1642955e-01f,9.078861e-01f,-4.192169e-01f,9.065957e-01f,-4.2200026e-01f,9.0529674e-01f,-4.2477968e-01f,
9.039893e-01f,-4.2755508e-01f,9.026733e-01f,-4.303265e-01f,9.013488e-01f,-4.3309382e-01f,9.000159e-01f,-4.358571e-01f,
8.986745e-01f,-4.3861625e-01f,8.9732456e-01f,-4.4137126e-01f,8.9596623e-01f,-4.4412214e-01f,8.945995e-01f,-4.4686884e-01f,
8.932243e-01f,-4.4961134e-01f,8.918407e-01f,-4.5234957e-01f,8.9044875e-01f,-4.5508358e-01f,8.8904834e-01f,-4.578133e-01f,
8.8763964e-01f,-4.6053872e-01f,8.8622254e-01f,-4.632598e-01f,8.847971e-01f,-4.659765e-01f,8.8336337e-01f,-4.6868882e-01f,
8.819213e-01f,-4.7139674e-01f,8.804709e-01f,-4.741002e-01f,8.790122e-01f,-4.7679922e-01f,8.775453e-01f,-4.7949377e-01f,
8.760701e-01f,-4.8218378e-01f,8.7458664e-01f,-4.8486924e-01f,8.73095e-01f,-4.8755017e-01f,8.715951e-01f,-4.9022648e-01f,
8.7008697e-01f,-4.928982e-01f,8.685707e-01f,-4.9556527e-01f,8.6704624e-01f,-4.9822766e-01f,8.655136e-01f,-5.0088537e-01f,
8.6397284e-01f,-5.0353837e-01f,8.6242396e-01f,-5.0618666e-01f,8.6086696e-01f,-5.0883013e-01f,8.593018e-01f,-5.114688e-01f,
8.577286e-01f,-5.1410276e-01f,8.5614735e-01f,-5.167318e-01f,8.54558e-01f,-5.19356e-01f,8.529606e-01f,-5.219753e-01f,
8.513552e-01f,-5.2458966e-01f,8.4974176e-01f,-5.2719915e-01f,8.4812033e-01f,-5.2980363e-01f,8.464909e-01f,-5.324031e-01f,
8.448536e-01f,-5.3499764e-01f,8.4320825e-01f,-5.3758705e-01f,8.41555e-01f,-5.4017144e-01f,8.398938e-01f,-5.427508e-01f,
8.382247e-01f,-5.45325e-01f,8.3654773e-01f,-5.4789406e-01f,8.348629e-01f,-5.5045795e-01f,8.331702e-01f,-5.530167e-01f,
8.314696e-01f,-5.5557024e-01f,8.297612e-01f,-5.581185e-01f,8.2804507e-01f,-5.6066155e-01f,8.2632107e-01f,-5.6319934e-01f,
8.245893e-01f,-5.657318e-01f,8.228498e-01f,-5.6825894e-01f,8.211025e-01f,-5.7078075e-01f,8.193475e-01f,-5.7329714e-01f,
8.175848e-01f,-5.7580817e-01f,8.1581444e-01f,-5.7831377e-01f,8.140363e-01f,-5.8081394e-01f,8.122506e-01f,-5.8330864e-01f,
8.1045717e-01f,-5.8579785e-01f,8.0865616e-01f,-5.882816e-01f,8.068476e-01f,-5.907597e-01f,8.0503136e-01f,-5.932323e-01f,
8.032075e-01f,-5.956993e-01f,8.0137616e-01f,-5.981607e-01f,7.9953724e-01f,-6.0061646e-01f,7.9769087e-01f,-6.030666e-01f,
7.958369e-01f,-6.0551107e-01f,7.939755e-01f,-6.079498e-01f,7.9210657e-01f,-6.103828e-01f,7.902302e-01f,-6.128101e-01f,
7.883464e-01f,-6.152316e-01f,7.864552e-01f,-6.176473e-01f,7.8455657e-01f,-6.200572e-01f,7.826506e-01f,-6.2246126e-01f,
7.807372e-01f,-6.248595e-01f,7.788165e-01f,-6.272518e-01f,7.768885e-01f,-6.2963825e-01f,7.749531e-01f,-6.3201874e-01f,
7.7301043e-01f,-6.343933e-01f,7.710605e-01f,-6.3676184e-01f,7.6910335e-01f,-6.3912445e-01f,7.671389e-01f,-6.4148104e-01f,
7.6516724e-01f,-6.4383155e-01f,7.631884e-01f,-6.4617604e-01f,7.612024e-01f,-6.485144e-01f,7.592092e-01f,-6.5084666e-01f,
7.572088e-01f,-6.5317285e-01f,7.552014e-01f,-6.5549284e-01f,7.531868e-01f,-6.578067e-01f,7.5116515e-01f,-6.6011435e-01f,
7.491364e-01f,-6.624158e-01f,7.471006e-01f,-6.64711e-01f,7.4505776e-01f,-6.6699994e-01f,7.4300796e-01f,-6.692826e-01f,
7.409511e-01f,-6.71559e-01f,7.388873e-01f,-6.73829e-01f,7.368166e-01f,-6.760927e-01f,7.347389e-01f,-6.7835003e-01f,
7.326543e-01f,-6.80601e-01f,7.3056275e-01f,-6.8284553e-01f,7.2846437e-01f,-6.850837e-01f,7.263591e-01f,-6.8731534e-01f,
7.242471e-01f,-6.8954057e-01f,7.221282e-01f,-6.917592e-01f,7.2000253e-01f,-6.9397146e-01f,7.1787006e-01f,-6.961771e-01f,
7.1573085e-01f,-6.9837624e-01f,7.1358484e-01f,-7.005688e-01f,7.114322e-01f,-7.0275474e-01f,7.092728e-01f,-7.0493406e-01f,
7.0710677e-01f,-7.0710677e-01f,7.0493406e-01f,-7.092728e-01f,7.0275474e-01f,-7.114322e-01f,7.005688e-01f,-7.1358484e-01f,
6.9837624e-01f,-7.1573085e-01f,6.961771e-01f,-7.1787006e-01f,6.9397146e-01f,-7.2000253e-01f,6.917592e-01f,-7.221282e-01f,
6.8954057e-01f,-7.242471e-01f,6.8731534e-01f,-7.263591e-01f,6.850837e-01f,-7.2846437e-01f,6.8284553e-01f,-7.3056275e-01f,
6.80601e-01f,-7.326543e-01f,6.7835003e-01f,-7.347389e-01f,6.760927e-01f,-7.368166e-01f,6.73829e-01f,-7.388873e-01f,
6.71559e-01f,-7.409511e-01f,6.692826e-01f,-7.4300796e-01f,6.6699994e-01f,-7.4505776e-01f,6.64711e-01f,-7.471006e-01f,
6.624158e-01f,-7.491364e-01f,6.6011435e-01f,-7.5116515e-01f,6.578067e-01f,-7.531868e-01f,6.5549284e-01f,-7.552014e-01f,
6.5317285e-01f,-7.572088e-01f,6.5084666e-01f,-7.592092e-01f,6.485144e-01f,-7.612024e-01f,6.4617604e-01f,-7.631884e-01f,
6.4383155e-01f,-7.6516724e-01f,6.4148104e-01f,-7.671389e-01f,6.3912445e-01f,-7.6910335e-01f,6.3676184e-01f,-7.710605e-01f,
6.343933e-01f,-7.7301043e-01f,6.3201874e-01f,-7.749531e-01f,6.2963825e-01f,-7.768885e-01f,6.272518e-01f,-7.788165e-01f,
6.248595e-01f,-7.807372e-01f,6.2246126e-01f,-7.826506e-01f,6.200572e-01f,-7.8455657e-01f,6.176473e-01f,-7.864552e-01f,
6.152316e-01f,-7.883464e-01f,6.128101e-01f,-7.902302e-01f,6.103828e-01f,-7.9210657e-01f,6.079498e-01f,-7.939755e-01f,
6.0551107e-01f,-7.958369e-01f,6.030666e-01f,-7.9769087e-01f,6.0061646e-01f,-7.9953724e-01f,5.981607e-01f,-8.0137616e-01f,
5.956993e-01f,-8.032075e-01f,5.932323e-01f,-8.0503136e-01f,5.907597e-01f,-8.068476e-01f,5.882816e-01f,-8.0865616e-01f,
5.8579785e-01f,-8.1045717e-01f,5.8330864e-01f,-8.122506e-01f,5.8081394e-01f,-8.140363e-01f,5.7831377e-01f,-8.1581444e-01f,
5.7580817e-01f,-8.175848e-01f,5.7329714e-01f,-8.193475e-01f,5.7078075e-01f,-8.211025e-01f,5.6825894e-01f,-8.228498e-01f,
5.657318e-01f,-8.245893e-01f,5.6319934e-01f,-8.2632107e-01f,5.6066155e-01f,-8.2804507e-01f,5.581185e-01f,-8.297612e-01f,
5.5557024e-01f,-8.314696e-01f,5.530167e-01f,-8.331702e-01f,5.5045795e-01f,-8.348629e-01f,5.4789406e-01f,-8.3654773e-01f,
5.45325e-01f,-8.382247e-01f,5.427508e-01f,-8.398938e-01f,5.4017144e-01f,-8.41555e-01f,5.3758705e-01f,-8.4320825e-01f,
5.3499764e-01f,-8.448536e-01f,5.324031e-01f,-8.464909e-01f,5.2980363e-01f,-8.4812033e-01f,5.2719915e-01f,-8.4974176e-01f,
5.2458966e-01f,-8.513552e-01f,5.219753e-01f,-8.529606e-01f,5.19356e-01f,-8.54558e-01f,5.167318e-01f,-8.5614735e-01f,
5.1410276e-01f,-8.577286e-01f,5.114688e-01f,-8.593018e-01f,5.0883013e-01f,-8.6086696e-01f,5.0618666e-01f,-8.6242396e-01f,
5.0353837e-01f,-8.6397284e-01f,5.0088537e-01f,-8.655136e-01f,4.9822766e-01f,-8.6704624e-01f,4.9556527e-01f,-8.685707e-01f,
4.928982e-01f,-8.7008697e-01f,4.9022648e-01f,-8.715951e-01f,4.8755017e-01f,-8.73095e-01f,4.8486924e-01f,-8.7458664e-01f,
4.8218378e-01f,-8.760701e-01f,4.7949377e-01f,-8.775453e-01f,4.7679922e-01f,-8.790122e-01f,4.741002e-01f,-8.804709e-01f,
4.7139674e-01f,-8.819213e-01f,4.6868882e-01f,-8.8336337e-01f,4.659765e-01f,-8.847971e-01f,4.632598e-01f,-8.8622254e-01f,
4.6053872e-01f,-8.8763964e-01f,4.578133e-01f,-8.8904834e-01f,4.5508358e-01f,-8.9044875e-01f,4.5234957e-01f,-8.918407e-01f,
4.4961134e-01f,-8.932243e-01f,4.4686884e-01f,-8.945995e-01f,4.4412214e-01f,-8.9596623e-01f,4.4137126e-01f,-8.9732456e-01f,
4.3861625e-01f,-8.986745e-01f,4.358571e-01f,-9.000159e-01f,4.3309382e-01f,-9.013488e-01f,4.303265e-01f,-9.026733e-01f,
4.2755508e-01f,-9.039893e-01f,4.2477968e-01f,-9.0529674e-01f,4.2200026e-01f,-9.065957e-01f,4.192169e-01f,-9.078861e-01f,
4.1642955e-01f,-9.09168e-01f,4.1363832e-01f,-9.104413e-01f,4.1084316e-01f,-9.1170603e-01f,4.0804416e-01f,-9.129622e-01f,
4.052413e-01f,-9.142098e-01f,4.0243465e-01f,-9.154487e-01f,3.996242e-01f,-9.166791e-01f,3.9681e-01f,-9.179008e-01f,
3.9399204e-01f,-9.191139e-01f,3.9117038e-01f,-9.203183e-01f,3.8834503e-01f,-9.2151403e-01f,3.8551605e-01f,-9.227011e-01f,
3.8268343e-01f,-9.238795e-01f,3.798472e-01f,-9.2504925e-01f,3.7700742e-01f,-9.262102e-01f,3.7416407e-01f,-9.273625e-01f,
3.713172e-01f,-9.285061e-01f,3.6846682e-01f,-9.296409e-01f,3.6561298e-01f,-9.3076694e-01f,3.6275572e-01f,-9.318843e-01f,
3.5989505e-01f,-9.329928e-01f,3.5703096e-01f,-9.340925e-01f,3.5416353e-01f,-9.351835e-01f,3.5129276e-01f,-9.3626565e-01f,
3.4841868e-01f,-9.37339e-01f,3.4554133e-01f,-9.3840355e-01f,3.4266073e-01f,-9.394592e-01f,3.3977687e-01f,-9.4050604e-01f,
3.3688986e-01f,-9.4154406e-01f,3.3399966e-01f,-9.425732e-01f,3.311063e-01f,-9.4359344e-01f,3.2820985e-01f,-9.446048e-01f,
3.253103e-01f,-9.456073e-01f,3.224077e-01f,-9.466009e-01f,3.1950203e-01f,-9.475856e-01f,3.1659338e-01f,-9.485614e-01f,
3.1368175e-01f,-9.4952816e-01f,3.1076714e-01f,-9.5048606e-01f,3.0784965e-01f,-9.51435e-01f,3.0492923e-01f,-9.52375e-01f,
3.0200595e-01f,-9.53306e-01f,2.9907984e-01f,-9.542281e-01f,2.961509e-01f,-9.551412e-01f,2.9321915e-01f,-9.5604527e-01f,
2.9028466e-01f,-9.5694035e-01f,2.8734747e-01f,-9.5782644e-01f,2.8440753e-01f,-9.5870346e-01f,2.8146493e-01f,-9.5957154e-01f,
2.785197e-01f,-9.604305e-01f,2.7557182e-01f,-9.6128047e-01f,2.7262136e-01f,-9.621214e-01f,2.696683e-01f,-9.6295327e-01f,
2.6671275e-01f,-9.6377605e-01f,2.6375467e-01f,-9.645898e-01f,2.607941e-01f,-9.6539444e-01f,2.578311e-01f,-9.6619e-01f,
2.5486565e-01f,-9.6697646e-01f,2.518978e-01f,-9.677538e-01f,2.4892761e-01f,-9.685221e-01f,2.4595505e-01f,-9.6928126e-01f,
2.4298018e-01f,-9.7003126e-01f,2.4000302e-01f,-9.7077215e-01f,2.370236e-01f,-9.715039e-01f,2.3404196e-01f,-9.722265e-01f,
2.310581e-01f,-9.7293997e-01f,2.2807208e-01f,-9.7364426e-01f,2.2508392e-01f,-9.7433937e-01f,2.2209363e-01f,-9.7502536e-01f,
2.1910124e-01f,-9.757021e-01f,2.161068e-01f,-9.7636974e-01f,2.1311031e-01f,-9.7702813e-01f,2.1011184e-01f,-9.7767735e-01f,
2.0711137e-01f,-9.783174e-01f,2.0410897e-01f,-9.789482e-01f,2.0110464e-01f,-9.795698e-01f,1.980984e-01f,-9.801821e-01f,
1.9509032e-01f,-9.8078525e-01f,1.920804e-01f,-9.813792e-01f,1.8906866e-01f,-9.819639e-01f,1.8605515e-01f,-9.825393e-01f,
1.8303989e-01f,-9.831055e-01f,1.800229e-01f,-9.836624e-01f,1.7700422e-01f,-9.842101e-01f,1.7398387e-01f,-9.847485e-01f,
1.7096189e-01f,-9.8527765e-01f,1.6793829e-01f,-9.857975e-01f,1.6491312e-01f,-9.863081e-01f,1.618864e-01f,-9.868094e-01f,
1.5885815e-01f,-9.873014e-01f,1.558284e-01f,-9.8778415e-01f,1.5279719e-01f,-9.882576e-01f,1.4976454e-01f,-9.8872167e-01f,
1.4673047e-01f,-9.891765e-01f,1.4369503e-01f,-9.89622e-01f,1.4065824e-01f,-9.900582e-01f,1.3762012e-01f,-9.904851e-01f,
1.345807e-01f,-9.9090266e-01f,1.3154003e-01f,-9.9131083e-01f,1.284981e-01f,-9.9170977e-01f,1.2545498e-01f,-9.920993e-01f,
1.2241068e-01f,-9.9247956e-01f,1.19365215e-01f,-9.928504e-01f,1.1631863e-01f,-9.932119e-01f,1.1327095e-01f,-9.935641e-01f,
1.10222206e-01f,-9.93907e-01f,1.0717242e-01f,-9.9424046e-01f,1.0412163e-01f,-9.945646e-01f,1.0106986e-01f,-9.948793e-01f,
9.801714e-02f,-9.951847e-01f,9.49635e-02f,-9.954808e-01f,9.1908954e-02f,-9.957674e-01f,8.885355e-02f,-9.960447e-01f,
8.579731e-02f,-9.963126e-01f,8.274026e-02f,-9.965711e-01f,7.968244e-02f,-9.968203e-01f,7.6623864e-02f,-9.9706006e-01f,
7.356457e-02f,-9.9729043e-01f,7.0504576e-02f,-9.9751145e-01f,6.744392e-02f,-9.9772304e-01f,6.438263e-02f,-9.979253e-01f,
6.1320737e-02f,-9.981181e-01f,5.8258265e-02f,-9.9830157e-01f,5.5195246e-02f,-9.9847555e-01f,5.2131705e-02f,-9.9864024e-01f,
4.9067676e-02f,-9.9879545e-01f,4.600318e-02f,-9.989413e-01f,4.293826e-02f,-9.9907774e-01f,3.9872926e-02f,-9.9920475e-01f,
3.6807224e-02f,-9.9932235e-01f,3.3741172e-02f,-9.994306e-01f,3.0674804e-02f,-9.995294e-01f,2.7608145e-02f,-9.996188e-01f,
2.4541229e-02f,-9.996988e-01f,2.147408e-02f,-9.997694e-01f,1.840673e-02f,-9.998306e-01f,1.5339206e-02f,-9.9988234e-01f,
1.2271538e-02f,-9.999247e-01f,9.203754e-03f,-9.999576e-01f,6.1358847e-03f,-9.9998116e-01f,3.0679568e-03f,-9.999953e-01f,
6.123234e-17f,-1.0e+00f,-3.0679568e-03f,-9.999953e-01f,-6.1358847e-03f,-9.9998116e-01f,-9.203754e-03f,-9.999576e-01f,
-1.2271538e-02f,-9.999247e-01f,-1.5339206e-02f,-9.9988234e-01f,-1.840673e-02f,-9.998306e-01f,-2.147408e-02f,-9.997694e-01f,
-2.4541229e-02f,-9.996988e-01f,-2.7608145e-02f,-9.996188e-01f,-3.0674804e-02f,-9.995294e-01f,-3.3741172e-02f,-9.994306e-01f,
-3.6807224e-02f,-9.9932235e-01f,-3.9872926e-02f,-9.9920475e-01f,-4.293826e-02f,-9.9907774e-01f,-4.600318e-02f,-9.989413e-01f,
-4.9067676e-02f,-9.9879545e-01f,-5.2131705e-02f,-9.9864024e-01f,-5.5195246e-02f,-9.9847555e-01f,-5.8258265e-02f,-9.9830157e-01f,
-6.1320737e-02f,-9.981181e-01f,-6.438263e-02f,-9.979253e-01f,-6.744392e-02f,-9.9772304e-01f,-7.0504576e-02f,-9.9751145e-01f,
-7.356457e-02f,-9.9729043e-01f,-7.6623864e-02f,-9.9706006e-01f,-7.968244e-02f,-9.968203e-01f,-8.274026e-02f,-9.965711e-01f,
-8.579731e-02f,-9.963126e-01f,-8.885355e-02f,-9.960447e-01f,-9.1908954e-02f,-9.957674e-01f,-9.49635e-02f,-9.954808e-01f,
-9.801714e-02f,-9.951847e-01f,-1.0106986e-01f,-9.948793e-01f,-1.0412163e-01f,-9.945646e-01f,-1.0717242e-01f,-9.9424046e-01f,
-1.10222206e-01f,-9.93907e-01f,-1.1327095e-01f,-9.935641e-01f,-1.1631863e-01f,-9.932119e-01f,-1.19365215e-01f,-9.928504e-01f,
-1.2241068e-01f,-9.9247956e-01f,-1.2545498e-01f,-9.920993e-01f,-1.284981e-01f,-9.9170977e-01f,-1.3154003e-01f,-9.9131083e-01f,
-1.345807e-01f,-9.9090266e-01f,-1.3762012e-01f,-9.904851e-01f,-1.4065824e-01f,-9.900582e-01f,-1.4369503e-01f,-9.89622e-01f,
-1.4673047e-01f,-9.891765e-01f,-1.4976454e-01f,-9.8872167e-01f,-1.5279719e-01f,-9.882576e-01f,-1.558284e-01f,-9.8778415e-01f,
-1.5885815e-01f,-9.873014e-01f,-1.618864e-01f,-9.868094e-01f,-1.6491312e-01f,-9.863081e-01f,-1.6793829e-01f,-9.857975e-01f,
-1.7096189e-01f,-9.8527765e-01f,-1.7398387e-01f,-9.847485e-01f,-1.7700422e-01f,-9.842101e-01f,-1.800229e-01f,-9.836624e-01f,
-1.8303989e-01f,-9.831055e-01f,-1.8605515e-01f,-9.825393e-01f,-1.8906866e-01f,-9.819639e-01f,-1.920804e-01f,-9.813792e-01f,
-1.9509032e-01f,-9.8078525e-01f,-1.980984e-01f,-9.801821e-01f,-2.0110464e-01f,-9.795698e-01f,-2.0410897e-01f,-9.789482e-01f,
-2.0711137e-01f,-9.783174e-01f,-2.1011184e-01f,-9.7767735e-01f,-2.1311031e-01f,-9.7702813e-01f,-2.161068e-01f,-9.7636974e-01f,
-2.1910124e-01f,-9.757021e-01f,-2.2209363e-01f,-9.7502536e-01f,-2.2508392e-01f,-9.7433937e-01f,-2.2807208e-01f,-9.7364426e-01f,
-2.310581e-01f,-9.7293997e-01f,-2.3404196e-01f,-9.722265e-01f,-2.370236e-01f,-9.715039e-01f,-2.4000302e-01f,-9.7077215e-01f,
-2.4298018e-01f,-9.7003126e-01f,-2.4595505e-01f,-9.6928126e-01f,-2.4892761e-01f,-9.685221e-01f,-2.518978e-01f,-9.677538e-01f,
-2.5486565e-01f,-9.6697646e-01f,-2.578311e-01f,-9.6619e-01f,-2.607941e-01f,-9.6539444e-01f,-2.6375467e-01f,-9.645898e-01f,
-2.6671275e-01f,-9.6377605e-01f,-2.696683e-01f,-9.6295327e-01f,-2.7262136e-01f,-9.621214e-01f,-2.7557182e-01f,-9.6128047e-01f,
-2.785197e-01f,-9.604305e-01f,-2.8146493e-01f,-9.5957154e-01f,-2.8440753e-01f,-9.5870346e-01f,-2.8734747e-01f,-9.5782644e-01f,
-2.9028466e-01f,-9.5694035e-01f,-2.9321915e-01f,-9.5604527e-01f,-2.961509e-01f,-9.551412e-01f,-2.9907984e-01f,-9.542281e-01f,
-3.0200595e-01f,-9.53306e-01f,-3.0492923e-01f,-9.52375e-01f,-3.0784965e-01f,-9.51435e-01f,-3.1076714e-01f,-9.5048606e-01f,
-3.1368175e-01f,-9.4952816e-01f,-3.1659338e-01f,-9.485614e-01f,-3.1950203e-01f,-9.475856e-01f,-3.224077e-01f,-9.466009e-01f,
-3.253103e-01f,-9.456073e-01f,-3.2820985e-01f,-9.446048e-01f,-3.311063e-01f,-9.4359344e-01f,-3.3399966e-01f,-9.425732e-01f,
-3.3688986e-01f,-9.4154406e-01f,-3.3977687e-01f,-9.4050604e-01f,-3.4266073e-01f,-9.394592e-01f,-3.4554133e-01f,-9.3840355e-01f,
-3.4841868e-01f,-9.37339e-01f,-3.5129276e-01f,-9.3626565e-01f,-3.5416353e-01f,-9.351835e-01f,-3.5703096e-01f,-9.340925e-01f,
-3.5989505e-01f,-9.329928e-01f,-3.6275572e-01f,-9.318843e-01f,-3.6561298e-01f,-9.3076694e-01f,-3.6846682e-01f,-9.296409e-01f,
-3.713172e-01f,-9.285061e-01f,-3.7416407e-01f,-9.273625e-01f,-3.7700742e-01f,-9.262102e-01f,-3.798472e-01f,-9.2504925e-01f,
-3.8268343e-01f,-9.238795e-01f,-3.8551605e-01f,-9.227011e-01f,-3.8834503e-01f,-9.2151403e-01f,-3.9117038e-01f,-9.203183e-01f,
-3.9399204e-01f,-9.191139e-01f,-3.9681e-01f,-9.179008e-01f,-3.996242e-01f,-9.166791e-01f,-4.0243465e-01f,-9.154487e-01f,
-4.052413e-01f,-9.142098e-01f,-4.0804416e-01f,-9.129622e-01f,-4.1084316e-01f,-9.1170603e-01f,-4.1363832e-01f,-9.104413e-01f,
-4.1642955e-01f,-9.09168e-01f,-4.192169e-01f,-9.078861e-01f,-4.2200026e-01f,-9.065957e-01f,-4.2477968e-01f,-9.0529674e-01f,
-4.2755508e-01f,-9.039893e-01f,-4.303265e-01f,-9.026733e-01f,-4.3309382e-01f,-9.013488e-01f,-4.358571e-01f,-9.000159e-01f,
-4.3861625e-01f,-8.986745e-01f,-4.4137126e-01f,-8.9732456e-01f,-4.4412214e-01f,-8.9596623e-01f,-4.4686884e-01f,-8.945995e-01f,
-4.4961134e-01f,-8.932243e-01f,-4.5234957e-01f,-8.918407e-01f,-4.5508358e-01f,-8.9044875e-01f,-4.578133e-01f,-8.8904834e-01f,
-4.6053872e-01f,-8.8763964e-01f,-4.632598e-01f,-8.8622254e-01f,-4.659765e-01f,-8.847971e-01f,-4.6868882e-01f,-8.8336337e-01f,
-4.7139674e-01f,-8.819213e-01f,-4.741002e-01f,-8.804709e-01f,-4.7679922e-01f,-8.790122e-01f,-4.7949377e-01f,-8.775453e-01f,
-4.8218378e-01f,-8.760701e-01f,-4.8486924e-01f,-8.7458664e-01f,-4.8755017e-01f,-8.73095e-01f,-4.9022648e-01f,-8.715951e-01f,
-4.928982e-01f,-8.7008697e-01f,-4.9556527e-01f,-8.685707e-01f,-4.9822766e-01f,-8.6704624e-01f,-5.0088537e-01f,-8.655136e-01f,
-5.0353837e-01f,-8.6397284e-01f,-5.0618666e-01f,-8.6242396e-01f,-5.0883013e-01f,-8.6086696e-01f,-5.114688e-01f,-8.593018e-01f,
-5.1410276e-01f,-8.577286e-01f,-5.167318e-01f,-8.5614735e-01f,-5.19356e-01f,-8.54558e-01f,-5.219753e-01f,-8.529606e-01f,
-5.2458966e-01f,-8.513552e-01f,-5.2719915e-01f,-8.4974176e-01f,-5.2980363e-01f,-8.4812033e-01f,-5.324031e-01f,-8.464909e-01f,
-5.3499764e-01f,-8.448536e-01f,-5.3758705e-01f,-8.4320825e-01f,-5.4017144e-01f,-8.41555e-01f,-5.427508e-01f,-8.398938e-01f,
-5.45325e-01f,-8.382247e-01f,-5.4789406e-01f,-8.3654773e-01f,-5.5045795e-01f,-8.348629e-01f,-5.530167e-01f,-8.331702e-01f,
-5.5557024e-01f,-8.314696e-01f,-5.581185e-01f,-8.297612e-01f,-5.6066155e-01f,-8.2804507e-01f,-5.6319934e-01f,-8.2632107e-01f,
-5.657318e-01f,-8.245893e-01f,-5.6825894e-01f,-8.228498e-01f,-5.7078075e-01f,-8.211025e-01f,-5.7329714e-01f,-8.193475e-01f,
-5.7580817e-01f,-8.175848e-01f,-5.7831377e-01f,-8.1581444e-01f,-5.8081394e-01f,-8.140363e-01f,-5.8330864e-01f,-8.122506e-01f,
-5.8579785e-01f,-8.1045717e-01f,-5.882816e-01f,-8.0865616e-01f,-5.907597e-01f,-8.068476e-01f,-5.932323e-01f,-8.0503136e-01f,
-5.956993e-01f,-8.032075e-01f,-5.981607e-01f,-8.0137616e-01f,-6.0061646e-01f,-7.9953724e-01f,-6.030666e-01f,-7.9769087e-01f,
-6.0551107e-01f,-7.958369e-01f,-6.079498e-01f,-7.939755e-01f,-6.103828e-01f,-7.9210657e-01f,-6.128101e-01f,-7.902302e-01f,
-6.152316e-01f,-7.883464e-01f,-6.176473e-01f,-7.864552e-01f,-6.200572e-01f,-7.8455657e-01f,-6.2246126e-01f,-7.826506e-01f,
-6.248595e-01f,-7.807372e-01f,-6.272518e-01f,-7.788165e-01f,-6.2963825e-01f,-7.768885e-01f,-6.3201874e-01f,-7.749531e-01f,
-6.343933e-01f,-7.7301043e-01f,-6.3676184e-01f,-7.710605e-01f,-6.3912445e-01f,-7.6910335e-01f,-6.4148104e-01f,-7.671389e-01f,
-6.4383155e-01f,-7.6516724e-01f,-6.4617604e-01f,-7.631884e-01f,-6.485144e-01f,-7.612024e-01f,-6.5084666e-01f,-7.592092e-01f,
-6.5317285e-01f,-7.572088e-01f,-6.5549284e-01f,-7.552014e-01f,-6.578067e-01f,-7.531868e-01f,-6.6011435e-01f,-7.5116515e-01f,
-6.624158e-01f,-7.491364e-01f,-6.64711e-01f,-7.471006e-01f,-6.6699994e-01f,-7.4505776e-01f,-6.692826e-01f,-7.4300796e-01f,
-6.71559e-01f,-7.409511e-01f,-6.73829e-01f,-7.388873e-01f,-6.760927e-01f,-7.368166e-01f,-6.7835003e-01f,-7.347389e-01f,
-6.80601e-01f,-7.326543e-01f,-6.8284553e-01f,-7.3056275e-01f,-6.850837e-01f,-7.2846437e-01f,-6.8731534e-01f,-7.263591e-01f,
-6.8954057e-01f,-7.242471e-01f,-6.917592e-01f,-7.221282e-01f,-6.9397146e-01f,-7.2000253e-01f,-6.961771e-01f,-7.1787006e-01f,
-6.9837624e-01f,-7.1573085e-01f,-7.005688e-01f,-7.1358484e-01f,-7.0275474e-01f,-7.114322e-01f,-7.0493406e-01f,-7.092728e-01f,
-7.0710677e-01f,-7.0710677e-01f,-7.092728e-01f,-7.0493406e-01f,-7.114322e-01f,-7.0275474e-01f,-7.1358484e-01f,-7.005688e-01f,
-7.1573085e-01f,-6.9837624e-01f,-7.1787006e-01f,-6.961771e-01f,-7.2000253e-01f,-6.9397146e-01f,-7.221282e-01f,-6.917592e-01f,
-7.242471e-01f,-6.8954057e-01f,-7.263591e-01f,-6.8731534e-01f,-7.2846437e-01f,-6.850837e-01f,-7.3056275e-01f,-6.8284553e-01f,
-7.326543e-01f,-6.80601e-01f,-7.347389e-01f,-6.7835003e-01f,-7.368166e-01f,-6.760927e-01f,-7.388873e-01f,-6.73829e-01f,
-7.409511e-01f,-6.71559e-01f,-7.4300796e-01f,-6.692826e-01f,-7.4505776e-01f,-6.6699994e-01f,-7.471006e-01f,-6.64711e-01f,
-7.491364e-01f,-6.624158e-01f,-7.5116515e-01f,-6.6011435e-01f,-7.531868e-01f,-6.578067e-01f,-7.552014e-01f,-6.5549284e-01f,
-7.572088e-01f,-6.5317285e-01f,-7.592092e-01f,-6.5084666e-01f,-7.612024e-01f,-6.485144e-01f,-7.631884e-01f,-6.4617604e-01f,
-7.6516724e-01f,-6.4383155e-01f,-7.671389e-01f,-6.4148104e-01f,-7.6910335e-01f,-6.3912445e-01f,-7.710605e-01f,-6.3676184e-01f,
-7.7301043e-01f,-6.343933e-01f,-7.749531e-01f,-6.3201874e-01f,-7.768885e-01f,-6.2963825e-01f,-7.788165e-01f,-6.272518e-01f,
-7.807372e-01f,-6.248595e-01f,-7.826506e-01f,-6.2246126e-01f,-7.8455657e-01f,-6.200572e-01f,-7.864552e-01f,-6.176473e-01f,
-7.883464e-01f,-6.152316e-01f,-7.902302e-01f,-6.128101e-01f,-7.9210657e-01f,-6.103828e-01f,-7.939755e-01f,-6.079498e-01f,
-7.958369e-01f,-6.0551107e-01f,-7.9769087e-01f,-6.030666e-01f,-7.9953724e-01f,-6.0061646e-01f,-8.0137616e-01f,-5.981607e-01f,
-8.032075e-01f,-5.956993e-01f,-8.0503136e-01f,-5.932323e-01f,-8.068476e-01f,-5.907597e-01f,-8.0865616e-01f,-5.882816e-01f,
-8.1045717e-01f,-5.8579785e-01f,-8.122506e-01f,-5.8330864e-01f,-8.140363e-01f,-5.8081394e-01f,-8.1581444e-01f,-5.7831377e-01f,
-8.175848e-01f,-5.7580817e-01f,-8.193475e-01f,-5.7329714e-01f,-8.211025e-01f,-5.7078075e-01f,-8.228498e-01f,-5.6825894e-01f,
-8.245893e-01f,-5.657318e-01f,-8.2632107e-01f,-5.6319934e-01f,-8.2804507e-01f,-5.6066155e-01f,-8.297612e-01f,-5.581185e-01f,
-8.314696e-01f,-5.5557024e-01f,-8.331702e-01f,-5.530167e-01f,-8.348629e-01f,-5.5045795e-01f,-8.3654773e-01f,-5.4789406e-01f,
-8.382247e-01f,-5.45325e-01f,-8.398938e-01f,-5.427508e-01f,-8.41555e-01f,-5.4017144e-01f,-8.4320825e-01f,-5.3758705e-01f,
-8.448536e-01f,-5.3499764e-01f,-8.464909e-01f,-5.324031e-01f,-8.4812033e-01f,-5.2980363e-01f,-8.4974176e-01f,-5.2719915e-01f,
-8.513552e-01f,-5.2458966e-01f,-8.529606e-01f,-5.219753e-01f,-8.54558e-01f,-5.19356e-01f,-8.5614735e-01f,-5.167318e-01f,
-8.577286e-01f,-5.1410276e-01f,-8.593018e-01f,-5.114688e-01f,-8.6086696e-01f,-5.0883013e-01f,-8.6242396e-01f,-5.0618666e-01f,
-8.6397284e-01f,-5.0353837e-01f,-8.655136e-01f,-5.0088537e-01f,-8.6704624e-01f,-4.9822766e-01f,-8.685707e-01f,-4.9556527e-01f,
-8.7008697e-01f,-4.928982e-01f,-8.715951e-01f,-4.9022648e-01f,-8.73095e-01f,-4.8755017e-01f,-8.7458664e-01f,-4.8486924e-01f,
-8.760701e-01f,-4.8218378e-01f,-8.775453e-01f,-4.7949377e-01f,-8.790122e-01f,-4.7679922e-01f,-8.804709e-01f,-4.741002e-01f,
-8.819213e-01f,-4.7139674e-01f,-8.8336337e-01f,-4.6868882e-01f,-8.847971e-01f,-4.659765e-01f,-8.8622254e-01f,-4.632598e-01f,
-8.8763964e-01f,-4.6053872e-01f,-8.8904834e-01f,-4.578133e-01f,-8.9044875e-01f,-4.5508358e-01f,-8.918407e-01f,-4.5234957e-01f,
-8.932243e-01f,-4.4961134e-01f,-8.945995e-01f,-4.4686884e-01f,-8.9596623e-01f,-4.4412214e-01f,-8.9732456e-01f,-4.4137126e-01f,
-8.986745e-01f,-4.3861625e-01f,-9.000159e-01f,-4.358571e-01f,-9.013488e-01f,-4.3309382e-01f,-9.026733e-01f,-4.303265e-01f,
-9.039893e-01f,-4.2755508e-01f,-9.0529674e-01f,-4.2477968e-01f,-9.065957e-01f,-4.2200026e-01f,-9.078861e-01f,-4.192169e-01f,
-9.09168e-01f,-4.1642955e-01f,-9.104413e-01f,-4.1363832e-01f,-9.1170603e-01f,-4.1084316e-01f,-9.129622e-01f,-4.0804416e-01f,
-9.142098e-01f,-4.052413e-01f,-9.154487e-01f,-4.0243465e-01f,-9.166791e-01f,-3.996242e-01f,-9.179008e-01f,-3.9681e-01f,
-9.191139e-01f,-3.9399204e-01f,-9.203183e-01f,-3.9117038e-01f,-9.2151403e-01f,-3.8834503e-01f,-9.227011e-01f,-3.8551605e-01f,
-9.238795e-01f,-3.8268343e-01f,-9.2504925e-01f,-3.798472e-01f,-9.262102e-01f,-3.7700742e-01f,-9.273625e-01f,-3.7416407e-01f,
-9.285061e-01f,-3.713172e-01f,-9.296409e-01f,-3.6846682e-01f,-9.3076694e-01f,-3.6561298e-01f,-9.318843e-01f,-3.6275572e-01f,
-9.329928e-01f,-3.5989505e-01f,-9.340925e-01f,-3.5703096e-01f,-9.351835e-01f,-3.5416353e-01f,-9.3626565e-01f,-3.5129276e-01f,
-9.37339e-01f,-3.4841868e-01f,-9.3840355e-01f,-3.4554133e-01f,-9.394592e-01f,-3.4266073e-01f,-9.4050604e-01f,-3.3977687e-01f,
-9.4154406e-01f,-3.3688986e-01f,-9.425732e-01f,-3.3399966e-01f,-9.4359344e-01f,-3.311063e-01f,-9.446048e-01f,-3.2820985e-01f,
-9.456073e-01f,-3.253103e-01f,-9.466009e-01f,-3.224077e-01f,-9.475856e-01f,-3.1950203e-01f,-9.485614e-01f,-3.1659338e-01f,
-9.4952816e-01f,-3.1368175e-01f,-9.5048606e-01f,-3.1076714e-01f,-9.51435e-01f,-3.0784965e-01f,-9.52375e-01f,-3.0492923e-01f,
-9.53306e-01f,-3.0200595e-01f,-9.542281e-01f,-2.9907984e-01f,-9.551412e-01f,-2.961509e-01f,-9.5604527e-01f,-2.9321915e-01f,
-9.5694035e-01f,-2.9028466e-01f,-9.5782644e-01f,-2.8734747e-01f,-9.5870346e-01f,-2.8440753e-01f,-9.5957154e-01f,-2.8146493e-01f,
-9.604305e-01f,-2.785197e-01f,-9.6128047e-01f,-2.7557182e-01f,-9.621214e-01f,-2.7262136e-01f,-9.6295327e-01f,-2.696683e-01f,
-9.6377605e-01f,-2.6671275e-01f,-9.645898e-01f,-2.6375467e-01f,-9.6539444e-01f,-2.607941e-01f,-9.6619e-01f,-2.578311e-01f,
-9.6697646e-01f,-2.5486565e-01f,-9.677538e-01f,-2.518978e-01f,-9.685221e-01f,-2.4892761e-01f,-9.6928126e-01f,-2.4595505e-01f,
-9.7003126e-01f,-2.4298018e-01f,-9.7077215e-01f,-2.4000302e-01f,-9.715039e-01f,-2.370236e-01f,-9.722265e-01f,-2.3404196e-01f,
-9.7293997e-01f,-2.310581e-01f,-9.7364426e-01f,-2.2807208e-01f,-9.7433937e-01f,-2.2508392e-01f,-9.7502536e-01f,-2.2209363e-01f,
-9.757021e-01f,-2.1910124e-01f,-9.7636974e-01f,-2.161068e-01f,-9.7702813e-01f,-2.1311031e-01f,-9.7767735e-01f,-2.1011184e-01f,
-9.783174e-01f,-2.0711137e-01f,-9.789482e-01f,-2.0410897e-01f,-9.795698e-01f,-2.0110464e-01f,-9.801821e-01f,-1.980984e-01f,
-9.8078525e-01f,-1.9509032e-01f,-9.813792e-01f,-1.920804e-01f,-9.819639e-01f,-1.8906866e-01f,-9.825393e-01f,-1.8605515e-01f,
-9.831055e-01f,-1.8303989e-01f,-9.836624e-01f,-1.800229e-01f,-9.842101e-01f,-1.7700422e-01f,-9.847485e-01f,-1.7398387e-01f,
-9.8527765e-01f,-1.7096189e-01f,-9.857975e-01f,-1.6793829e-01f,-9.863081e-01f,-1.6491312e-01f,-9.868094e-01f,-1.618864e-01f,
-9.873014e-01f,-1.5885815e-01f,-9.8778415e-01f,-1.558284e-01f,-9.882576e-01f,-1.5279719e-01f,-9.8872167e-01f,-1.4976454e-01f,
-9.891765e-01f,-1.4673047e-01f,-9.89622e-01f,-1.4369503e-01f,-9.900582e-01f,-1.4065824e-01f,-9.904851e-01f,-1.3762012e-01f,
-9.9090266e-01f,-1.345807e-01f,-9.9131083e-01f,-1.3154003e-01f,-9.9170977e-01f,-1.284981e-01f,-9.920993e-01f,-1.2545498e-01f,
-9.9247956e-01f,-1.2241068e-01f,-9.928504e-01f,-1.19365215e-01f,-9.932119e-01f,-1.1631863e-01f,-9.935641e-01f,-1.1327095e-01f,
-9.93907e-01f,-1.10222206e-01f,-9.9424046e-01f,-1.0717242e-01f,-9.945646e-01f,-1.0412163e-01f,-9.948793e-01f,-1.0106986e-01f,
-9.951847e-01f,-9.801714e-02f,-9.954808e-01f,-9.49635e-02f,-9.957674e-01f,-9.1908954e-02f,-9.960447e-01f,-8.885355e-02f,
-9.963126e-01f,-8.579731e-02f,-9.965711e-01f,-8.274026e-02f,-9.968203e-01f,-7.968244e-02f,-9.9706006e-01f,-7.6623864e-02f,
-9.9729043e-01f,-7.356457e-02f,-9.9751145e-01f,-7.0504576e-02f,-9.9772304e-01f,-6.744392e-02f,-9.979253e-01f,-6.438263e-02f,
-9.981181e-01f,-6.1320737e-02f,-9.9830157e-01f,-5.8258265e-02f,-9.9847555e-01f,-5.5195246e-02f,-9.9864024e-01f,-5.2131705e-02f,
-9.9879545e-01f,-4.9067676e-02f,-9.989413e-01f,-4.600318e-02f,-9.9907774e-01f,-4.293826e-02f,-9.9920475e-01f,-3.9872926e-02f,
-9.9932235e-01f,-3.6807224e-02f,-9.994306e-01f,-3.3741172e-02f,-9.995294e-01f,-3.0674804e-02f,-9.996188e-01f,-2.7608145e-02f,
-9.996988e-01f,-2.4541229e-02f,-9.997694e-01f,-2.147408e-02f,-9.998306e-01f,-1.840673e-02f,-9.9988234e-01f,-1.5339206e-02f,
-9.999247e-01f,-1.2271538e-02f,-9.999576e-01f,-9.203754e-03f,-9.9998116e-01f,-6.1358847e-03f,-9.999953e-01f,-3.0679568e-03f,
-1.0e+00f,-1.2246469e-16f,-9.999953e-01f,3.0679568e-03f,-9.9998116e-01f,6.1358847e-03f,-9.999576e-01f,9.203754e-03f,
-9.999247e-01f,1.2271538e-02f,-9.9988234e-01f,1.5339206e-02f,-9.998306e-01f,1.840673e-02f,-9.997694e-01f,2.147408e-02f,
-9.996988e-01f,2.4541229e-02f,-9.996188e-01f,2.7608145e-02f,-9.995294e-01f,3.0674804e-02f,-9.994306e-01f,3.3741172e-02f,
-9.9932235e-01f,3.6807224e-02f,-9.9920475e-01f,3.9872926e-02f,-9.9907774e-01f,4.293826e-02f,-9.989413e-01f,4.600318e-02f,
-9.9879545e-01f,4.9067676e-02f,-9.9864024e-01f,5.2131705e-02f,-9.9847555e-01f,5.5195246e-02f,-9.9830157e-01f,5.8258265e-02f,
-9.981181e-01f,6.1320737e-02f,-9.979253e-01f,6.438263e-02f,-9.9772304e-01f,6.744392e-02f,-9.9751145e-01f,7.0504576e-02f,
-9.9729043e-01f,7.356457e-02f,-9.9706006e-01f,7.6623864e-02f,-9.968203e-01f,7.968244e-02f,-9.965711e-01f,8.274026e-02f,
-9.963126e-01f,8.579731e-02f,-9.960447e-01f,8.885355e-02f,-9.957674e-01f,9.1908954e-02f,-9.954808e-01f,9.49635e-02f,
-9.951847e-01f,9.801714e-02f,-9.948793e-01f,1.0106986e-01f,-9.945646e-01f,1.0412163e-01f,-9.9424046e-01f,1.0717242e-01f,
-9.93907e-01f,1.10222206e-01f,-9.935641e-01f,1.1327095e-01f,-9.932119e-01f,1.1631863e-01f,-9.928504e-01f,1.19365215e-01f,
-9.9247956e-01f,1.2241068e-01f,-9.920993e-01f,1.2545498e-01f,-9.9170977e-01f,1.284981e-01f,-9.9131083e-01f,1.3154003e-01f,
-9.9090266e-01f,1.345807e-01f,-9.904851e-01f,1.3762012e-01f,-9.900582e-01f,1.4065824e-01f,-9.89622e-01f,1.4369503e-01f,
-9.891765e-01f,1.4673047e-01f,-9.8872167e-01f,1.4976454e-01f,-9.882576e-01f,1.5279719e-01f,-9.8778415e-01f,1.558284e-01f,
-9.873014e-01f,1.5885815e-01f,-9.868094e-01f,1.618864e-01f,-9.863081e-01f,1.6491312e-01f,-9.857975e-01f,1.6793829e-01f,
-9.8527765e-01f,1.7096189e-01f,-9.847485e-01f,1.7398387e-01f,-9.842101e-01f,1.7700422e-01f,-9.836624e-01f,1.800229e-01f,
-9.831055e-01f,1.8303989e-01f,-9.825393e-01f,1.8605515e-01f,-9.819639e-01f,1.8906866e-01f,-9.813792e-01f,1.920804e-01f,
-9.8078525e-01f,1.9509032e-01f,-9.801821e-01f,1.980984e-01f,-9.795698e-01f,2.0110464e-01f,-9.789482e-01f,2.0410897e-01f,
-9.783174e-01f,2.0711137e-01f,-9.7767735e-01f,2.1011184e-01f,-9.7702813e-01f,2.1311031e-01f,-9.7636974e-01f,2.161068e-01f,
-9.757021e-01f,2.1910124e-01f,-9.7502536e-01f,2.2209363e-01f,-9.7433937e-01f,2.2508392e-01f,-9.7364426e-01f,2.2807208e-01f,
-9.7293997e-01f,2.310581e-01f,-9.722265e-01f,2.3404196e-01f,-9.715039e-01f,2.370236e-01f,-9.7077215e-01f,2.4000302e-01f,
-9.7003126e-01f,2.4298018e-01f,-9.6928126e-01f,2.4595505e-01f,-9.685221e-01f,2.4892761e-01f,-9.677538e-01f,2.518978e-01f,
-9.6697646e-01f,2.5486565e-01f,-9.6619e-01f,2.578311e-01f,-9.6539444e-01f,2.607941e-01f,-9.645898e-01f,2.6375467e-01f,
-9.6377605e-01f,2.6671275e-01f,-9.6295327e-01f,2.696683e-01f,-9.621214e-01f,2.7262136e-01f,-9.6128047e-01f,2.7557182e-01f,
-9.604305e-01f,2.785197e-01f,-9.5957154e-01f,2.8146493e-01f,-9.5870346e-01f,2.8440753e-01f,-9.5782644e-01f,2.8734747e-01f,
-9.5694035e-01f,2.9028466e-01f,-9.5604527e-01f,2.9321915e-01f,-9.551412e-01f,2.961509e-01f,-9.542281e-01f,2.9907984e-01f,
-9.53306e-01f,3.0200595e-01f,-9.52375e-01f,3.0492923e-01f,-9.51435e-01f,3.0784965e-01f,-9.5048606e-01f,3.1076714e-01f,
-9.4952816e-01f,3.1368175e-01f,-9.485614e-01f,3.1659338e-01f,-9.475856e-01f,3.1950203e-01f,-9.466009e-01f,3.224077e-01f,
-9.456073e-01f,3.253103e-01f,-9.446048e-01f,3.2820985e-01f,-9.4359344e-01f,3.311063e-01f,-9.425732e-01f,3.3399966e-01f,
-9.4154406e-01f,3.3688986e-01f,-9.4050604e-01f,3.3977687e-01f,-9.394592e-01f,3.4266073e-01f,-9.3840355e-01f,3.4554133e-01f,
-9.37339e-01f,3.4841868e-01f,-9.3626565e-01f,3.5129276e-01f,-9.351835e-01f,3.5416353e-01f,-9.340925e-01f,3.5703096e-01f,
-9.329928e-01f,3.5989505e-01f,-9.318843e-01f,3.6275572e-01f,-9.3076694e-01f,3.6561298e-01f,-9.296409e-01f,3.6846682e-01f,
-9.285061e-01f,3.713172e-01f,-9.273625e-01f,3.7416407e-01f,-9.262102e-01f,3.7700742e-01f,-9.2504925e-01f,3.798472e-01f,
-9.238795e-01f,3.8268343e-01f,-9.227011e-01f,3.8551605e-01f,-9.2151403e-01f,3.8834503e-01f,-9.203183e-01f,3.9117038e-01f,
-9.191139e-01f,3.9399204e-01f,-9.179008e-01f,3.9681e-01f,-9.166791e-01f,3.996242e-01f,-9.154487e-01f,4.0243465e-01f,
-9.142098e-01f,4.052413e-01f,-9.129622e-01f,4.0804416e-01f,-9.1170603e-01f,4.1084316e-01f,-9.104413e-01f,4.1363832e-01f,
-9.09168e-01f,4.1642955e-01f,-9.078861e-01f,4.192169e-01f,-9.065957e-01f,4.2200026e-01f,-9.0529674e-01f,4.2477968e-01f,
-9.039893e-01f,4.2755508e-01f,-9.026733e-01f,4.303265e-01f,-9.013488e-01f,4.3309382e-01f,-9.000159e-01f,4.358571e-01f,
-8.986745e-01f,4.3861625e-01f,-8.9732456e-01f,4.4137126e-01f,-8.9596623e-01f,4.4412214e-01f,-8.945995e-01f,4.4686884e-01f,
-8.932243e-01f,4.4961134e-01f,-8.918407e-01f,4.5234957e-01f,-8.9044875e-01f,4.5508358e-01f,-8.8904834e-01f,4.578133e-01f,
-8.8763964e-01f,4.6053872e-01f,-8.8622254e-01f,4.632598e-01f,-8.847971e-01f,4.659765e-01f,-8.8336337e-01f,4.6868882e-01f,
-8.819213e-01f,4.7139674e-01f,-8.804709e-01f,4.741002e-01f,-8.790122e-01f,4.7679922e-01f,-8.775453e-01f,4.7949377e-01f,
-8.760701e-01f,4.8218378e-01f,-8.7458664e-01f,4.8486924e-01f,-8.73095e-01f,4.8755017e-01f,-8.715951e-01f,4.9022648e-01f,
-8.7008697e-01f,4.928982e-01f,-8.685707e-01f,4.9556527e-01f,-8.6704624e-01f,4.9822766e-01f,-8.655136e-01f,5.0088537e-01f,
-8.6397284e-01f,5.0353837e-01f,-8.6242396e-01f,5.0618666e-01f,-8.6086696e-01f,5.0883013e-01f,-8.593018e-01f,5.114688e-01f,
-8.577286e-01f,5.1410276e-01f,-8.5614735e-01f,5.167318e-01f,-8.54558e-01f,5.19356e-01f,-8.529606e-01f,5.219753e-01f,
-8.513552e-01f,5.2458966e-01f,-8.4974176e-01f,5.2719915e-01f,-8.4812033e-01f,5.2980363e-01f,-8.464909e-01f,5.324031e-01f,
-8.448536e-01f,5.3499764e-01f,-8.4320825e-01f,5.3758705e-01f,-8.41555e-01f,5.4017144e-01f,-8.398938e-01f,5.427508e-01f,
-8.382247e-01f,5.45325e-01f,-8.3654773e-01f,5.4789406e-01f,-8.348629e-01f,5.5045795e-01f,-8.331702e-01f,5.530167e-01f,
-8.314696e-01f,5.5557024e-01f,-8.297612e-01f,5.581185e-01f,-8.2804507e-01f,5.6066155e-01f,-8.2632107e-01f,5.6319934e-01f,
-8.245893e-01f,5.657318e-01f,-8.228498e-01f,5.6825894e-01f,-8.211025e-01f,5.7078075e-01f,-8.193475e-01f,5.7329714e-01f,
-8.175848e-01f,5.7580817e-01f,-8.1581444e-01f,5.7831377e-01f,-8.140363e-01f,5.8081394e-01f,-8.122506e-01f,5.8330864e-01f,
-8.1045717e-01f,5.8579785e-01f,-8.0865616e-01f,5.882816e-01f,-8.068476e-01f,5.907597e-01f,-8.0503136e-01f,5.932323e-01f,
-8.032075e-01f,5.956993e-01f,-8.0137616e-01f,5.981607e-01f,-7.9953724e-01f,6.0061646e-01f,-7.9769087e-01f,6.030666e-01f,
-7.958369e-01f,6.0551107e-01f,-7.939755e-01f,6.079498e-01f,-7.9210657e-01f,6.103828e-01f,-7.902302e-01f,6.128101e-01f,
-7.883464e-01f,6.152316e-01f,-7.864552e-01f,6.176473e-01f,-7.8455657e-01f,6.200572e-01f,-7.826506e-01f,6.2246126e-01f,
-7.807372e-01f,6.248595e-01f,-7.788165e-01f,6.272518e-01f,-7.768885e-01f,6.2963825e-01f,-7.749531e-01f,6.3201874e-01f,
-7.7301043e-01f,6.343933e-01f,-7.710605e-01f,6.3676184e-01f,-7.6910335e-01f,6.3912445e-01f,-7.671389e-01f,6.4148104e-01f,
-7.6516724e-01f,6.4383155e-01f,-7.631884e-01f,6.4617604e-01f,-7.612024e-01f,6.485144e-01f,-7.592092e-01f,6.5084666e-01f,
-7.572088e-01f,6.5317285e-01f,-7.552014e-01f,6.5549284e-01f,-7.531868e-01f,6.578067e-01f,-7.5116515e-01f,6.6011435e-01f,
-7.491364e-01f,6.624158e-01f,-7.471006e-01f,6.64711e-01f,-7.4505776e-01f,6.6699994e-01f,-7.4300796e-01f,6.692826e-01f,
-7.409511e-01f,6.71559e-01f,-7.388873e-01f,6.73829e-01f,-7.368166e-01f,6.760927e-01f,-7.347389e-01f,6.7835003e-01f,
-7.326543e-01f,6.80601e-01f,-7.3056275e-01f,6.8284553e-01f,-7.2846437e-01f,6.850837e-01f,-7.263591e-01f,6.8731534e-01f,
-7.242471e-01f,6.8954057e-01f,-7.221282e-01f,6.917592e-01f,-7.2000253e-01f,6.9397146e-01f,-7.1787006e-01f,6.961771e-01f,
-7.1573085e-01f,6.9837624e-01f,-7.1358484e-01f,7.005688e-01f,-7.114322e-01f,7.0275474e-01f,-7.092728e-01f,7.0493406e-01f,
-7.0710677e-01f,7.0710677e-01f,-7.0493406e-01f,7.092728e-01f,-7.0275474e-01f,7.114322e-01f,-7.005688e-01f,7.1358484e-01f,
-6.9837624e-01f,7.1573085e-01f,-6.961771e-01f,7.1787006e-01f,-6.9397146e-01f,7.2000253e-01f,-6.917592e-01f,7.221282e-01f,
-6.8954057e-01f,7.242471e-01f,-6.8731534e-01f,7.263591e-01f,-6.850837e-01f,7.2846437e-01f,-6.8284553e-01f,7.3056275e-01f,
-6.80601e-01f,7.326543e-01f,-6.7835003e-01f,7.347389e-01f,-6.760927e-01f,7.368166e-01f,-6.73829e-01f,7.388873e-01f,
-6.71559e-01f,7.409511e-01f,-6.692826e-01f,7.4300796e-01f,-6.6699994e-01f,7.4505776e-01f,-6.64711e-01f,7.471006e-01f,
-6.624158e-01f,7.491364e-01f,-6.6011435e-01f,7.5116515e-01f,-6.578067e-01f,7.531868e-01f,-6.5549284e-01f,7.552014e-01f,
-6.5317285e-01f,7.572088e-01f,-6.5084666e-01f,7.592092e-01f,-6.485144e-01f,7.612024e-01f,-6.4617604e-01f,7.631884e-01f,
-6.4383155e-01f,7.6516724e-01f,-6.4148104e-01f,7.671389e-01f,-6.3912445e-01f,7.6910335e-01f,-6.3676184e-01f,7.710605e-01f,
-6.343933e-01f,7.7301043e-01f,-6.3201874e-01f,7.749531e-01f,-6.2963825e-01f,7.768885e-01f,-6.272518e-01f,7.788165e-01f,
-6.248595e-01f,7.807372e-01f,-6.2246126e-01f,7.826506e-01f,-6.200572e-01f,7.8455657e-01f,-6.176473e-01f,7.864552e-01f,
-6.152316e-01f,7.883464e-01f,-6.128101e-01f,7.902302e-01f,-6.103828e-01f,7.9210657e-01f,-6.079498e-01f,7.939755e-01f,
-6.0551107e-01f,7.958369e-01f,-6.030666e-01f,7.9769087e-01f,-6.0061646e-01f,7.9953724e-01f,-5.981607e-01f,8.0137616e-01f,
-5.956993e-01f,8.032075e-01f,-5.932323e-01f,8.0503136e-01f,-5.907597e-01f,8.068476e-01f,-5.882816e-01f,8.0865616e-01f,
-5.8579785e-01f,8.1045717e-01f,-5.8330864e-01f,8.122506e-01f,-5.8081394e-01f,8.140363e-01f,-5.7831377e-01f,8.1581444e-01f,
-5.7580817e-01f,8.175848e-01f,-5.7329714e-01f,8.193475e-01f,-5.7078075e-01f,8.211025e-01f,-5.6825894e-01f,8.228498e-01f,
-5.657318e-01f,8.245893e-01f,-5.6319934e-01f,8.2632107e-01f,-5.6066155e-01f,8.2804507e-01f,-5.581185e-01f,8.297612e-01f,
-5.5557024e-01f,8.314696e-01f,-5.530167e-01f,8.331702e-01f,-5.5045795e-01f,8.348629e-01f,-5.4789406e-01f,8.3654773e-01f,
-5.45325e-01f,8.382247e-01f,-5.427508e-01f,8.398938e-01f,-5.4017144e-01f,8.41555e-01f,-5.3758705e-01f,8.4320825e-01f,
-5.3499764e-01f,8.448536e-01f,-5.324031e-01f,8.464909e-01f,-5.2980363e-01f,8.4812033e-01f,-5.2719915e-01f,8.4974176e-01f,
-5.2458966e-01f,8.513552e-01f,-5.219753e-01f,8.529606e-01f,-5.19356e-01f,8.54558e-01f,-5.167318e-01f,8.5614735e-01f,
-5.1410276e-01f,8.577286e-01f,-5.114688e-01f,8.593018e-01f,-5.0883013e-01f,8.6086696e-01f,-5.0618666e-01f,8.6242396e-01f,
-5.0353837e-01f,8.6397284e-01f,-5.0088537e-01f,8.655136e-01f,-4.9822766e-01f,8.6704624e-01f,-4.9556527e-01f,8.685707e-01f,
-4.928982e-01f,8.7008697e-01f,-4.9022648e-01f,8.715951e-01f,-4.8755017e-01f,8.73095e-01f,-4.8486924e-01f,8.7458664e-01f,
-4.8218378e-01f,8.760701e-01f,-4.7949377e-01f,8.775453e-01f,-4.7679922e-01f,8.790122e-01f,-4.741002e-01f,8.804709e-01f,
-4.7139674e-01f,8.819213e-01f,-4.6868882e-01f,8.8336337e-01f,-4.659765e-01f,8.847971e-01f,-4.632598e-01f,8.8622254e-01f,
-4.6053872e-01f,8.8763964e-01f,-4.578133e-01f,8.8904834e-01f,-4.5508358e-01f,8.9044875e-01f,-4.5234957e-01f,8.918407e-01f,
-4.4961134e-01f,8.932243e-01f,-4.4686884e-01f,8.945995e-01f,-4.4412214e-01f,8.9596623e-01f,-4.4137126e-01f,8.9732456e-01f,
-4.3861625e-01f,8.986745e-01f,-4.358571e-01f,9.000159e-01f,-4.3309382e-01f,9.013488e-01f,-4.303265e-01f,9.026733e-01f,
-4.2755508e-01f,9.039893e-01f,-4.2477968e-01f,9.0529674e-01f,-4.2200026e-01f,9.065957e-01f,-4.192169e-01f,9.078861e-01f,
-4.1642955e-01f,9.09168e-01f,-4.1363832e-01f,9.104413e-01f,-4.1084316e-01f,9.1170603e-01f,-4.0804416e-01f,9.129622e-01f,
-4.052413e-01f,9.142098e-01f,-4.0243465e-01f,9.154487e-01f,-3.996242e-01f,9.166791e-01f,-3.9681e-01f,9.179008e-01f,
-3.9399204e-01f,9.191139e-01f,-3.9117038e-01f,9.203183e-01f,-3.8834503e-01f,9.2151403e-01f,-3.8551605e-01f,9.227011e-01f,
-3.8268343e-01f,9.238795e-01f,-3.798472e-01f,9.2504925e-01f,-3.7700742e-01f,9.262102e-01f,-3.7416407e-01f,9.273625e-01f,
-3.713172e-01f,9.285061e-01f,-3.6846682e-01f,9.296409e-01f,-3.6561298e-01f,9.3076694e-01f,-3.6275572e-01f,9.318843e-01f,
-3.5989505e-01f,9.329928e-01f,-3.5703096e-01f,9.340925e-01f,-3.5416353e-01f,9.351835e-01f,-3.5129276e-01f,9.3626565e-01f,
-3.4841868e-01f,9.37339e-01f,-3.4554133e-01f,9.3840355e-01f,-3.4266073e-01f,9.394592e-01f,-3.3977687e-01f,9.4050604e-01f,
-3.3688986e-01f,9.4154406e-01f,-3.3399966e-01f,9.425732e-01f,-3.311063e-01f,9.4359344e-01f,-3.2820985e-01f,9.446048e-01f,
-3.253103e-01f,9.456073e-01f,-3.224077e-01f,9.466009e-01f,-3.1950203e-01f,9.475856e-01f,-3.1659338e-01f,9.485614e-01f,
-3.1368175e-01f,9.4952816e-01f,-3.1076714e-01f,9.5048606e-01f,-3.0784965e-01f,9.51435e-01f,-3.0492923e-01f,9.52375e-01f,
-3.0200595e-01f,9.53306e-01f,-2.9907984e-01f,9.542281e-01f,-2.961509e-01f,9.551412e-01f,-2.9321915e-01f,9.5604527e-01f,
-2.9028466e-01f,9.5694035e-01f,-2.8734747e-01f,9.5782644e-01f,-2.8440753e-01f,9.5870346e-01f,-2.8146493e-01f,9.5957154e-01f,
-2.785197e-01f,9.604305e-01f,-2.7557182e-01f,9.6128047e-01f,-2.7262136e-01f,9.621214e-01f,-2.696683e-01f,9.6295327e-01f,
-2.6671275e-01f,9.6377605e-01f,-2.6375467e-01f,9.645898e-01f,-2.607941e-01f,9.6539444e-01f,-2.578311e-01f,9.6619e-01f,
-2.5486565e-01f,9.6697646e-01f,-2.518978e-01f,9.677538e-01f,-2.4892761e-01f,9.685221e-01f,-2.4595505e-01f,9.6928126e-01f,
-2.4298018e-01f,9.7003126e-01f,-2.4000302e-01f,9.7077215e-01f,-2.370236e-01f,9.715039e-01f,-2.3404196e-01f,9.722265e-01f,
-2.310581e-01f,9.7293997e-01f,-2.2807208e-01f,9.7364426e-01f,-2.2508392e-01f,9.7433937e-01f,-2.2209363e-01f,9.7502536e-01f,
-2.1910124e-01f,9.757021e-01f,-2.161068e-01f,9.7636974e-01f,-2.1311031e-01f,9.7702813e-01f,-2.1011184e-01f,9.7767735e-01f,
-2.0711137e-01f,9.783174e-01f,-2.0410897e-01f,9.789482e-01f,-2.0110464e-01f,9.795698e-01f,-1.980984e-01f,9.801821e-01f,
-1.9509032e-01f,9.8078525e-01f,-1.920804e-01f,9.813792e-01f,-1.8906866e-01f,9.819639e-01f,-1.8605515e-01f,9.825393e-01f,
-1.8303989e-01f,9.831055e-01f,-1.800229e-01f,9.836624e-01f,-1.7700422e-01f,9.842101e-01f,-1.7398387e-01f,9.847485e-01f,
-1.7096189e-01f,9.8527765e-01f,-1.6793829e-01f,9.857975e-01f,-1.6491312e-01f,9.863081e-01f,-1.618864e-01f,9.868094e-01f,
-1.5885815e-01f,9.873014e-01f,-1.558284e-01f,9.8778415e-01f,-1.5279719e-01f,9.882576e-01f,-1.4976454e-01f,9.8872167e-01f,
-1.4673047e-01f,9.891765e-01f,-1.4369503e-01f,9.89622e-01f,-1.4065824e-01f,9.900582e-01f,-1.3762012e-01f,9.904851e-01f,
-1.345807e-01f,9.9090266e-01f,-1.3154003e-01f,9.9131083e-01f,-1.284981e-01f,9.9170977e-01f,-1.2545498e-01f,9.920993e-01f,
-1.2241068e-01f,9.9247956e-01f,-1.19365215e-01f,9.928504e-01f,-1.1631863e-01f,9.932119e-01f,-1.1327095e-01f,9.935641e-01f,
-1.10222206e-01f,9.93907e-01f,-1.0717242e-01f,9.9424046e-01f,-1.0412163e-01f,9.945646e-01f,-1.0106986e-01f,9.948793e-01f,
-9.801714e-02f,9.951847e-01f,-9.49635e-02f,9.954808e-01f,-9.1908954e-02f,9.957674e-01f,-8.885355e-02f,9.960447e-01f,
-8.579731e-02f,9.963126e-01f,-8.274026e-02f,9.965711e-01f,-7.968244e-02f,9.968203e-01f,-7.6623864e-02f,9.9706006e-01f,
-7.356457e-02f,9.9729043e-01f,-7.0504576e-02f,9.9751145e-01f,-6.744392e-02f,9.9772304e-01f,-6.438263e-02f,9.979253e-01f,
-6.1320737e-02f,9.981181e-01f,-5.8258265e-02f,9.9830157e-01f,-5.5195246e-02f,9.9847555e-01f,-5.2131705e-02f,9.9864024e-01f,
-4.9067676e-02f,9.9879545e-01f,-4.600318e-02f,9.989413e-01f,-4.293826e-02f,9.9907774e-01f,-3.9872926e-02f,9.9920475e-01f,
-3.6807224e-02f,9.9932235e-01f,-3.3741172e-02f,9.994306e-01f,-3.0674804e-02f,9.995294e-01f,-2.7608145e-02f,9.996188e-01f,
-2.4541229e-02f,9.996988e-01f,-2.147408e-02f,9.997694e-01f,-1.840673e-02f,9.998306e-01f,-1.5339206e-02f,9.9988234e-01f,
-1.2271538e-02f,9.999247e-01f,-9.203754e-03f,9.999576e-01f,-6.1358847e-03f,9.9998116e-01f,-3.0679568e-03f,9.999953e-01f,
-1.8369701e-16f,1.0e+00f,3.0679568e-03f,9.999953e-01f,6.1358847e-03f,9.9998116e-01f,9.203754e-03f,9.999576e-01f,
1.2271538e-02f,9.999247e-01f,1.5339206e-02f,9.9988234e-01f,1.840673e-02f,9.998306e-01f,2.147408e-02f,9.997694e-01f,
2.4541229e-02f,9.996988e-01f,2.7608145e-02f,9.996188e-01f,3.0674804e-02f,9.995294e-01f,3.3741172e-02f,9.994306e-01f,
3.6807224e-02f,9.9932235e-01f,3.9872926e-02f,9.9920475e-01f,4.293826e-02f,9.9907774e-01f,4.600318e-02f,9.989413e-01f,
4.9067676e-02f,9.9879545e-01f,5.2131705e-02f,9.9864024e-01f,5.5195246e-02f,9.9847555e-01f,5.8258265e-02f,9.9830157e-01f,
6.1320737e-02f,9.981181e-01f,6.438263e-02f,9.979253e-01f,6.744392e-02f,9.9772304e-01f,7.0504576e-02f,9.9751145e-01f,
7.356457e-02f,9.9729043e-01f,7.6623864e-02f,9.9706006e-01f,7.968244e-02f,9.968203e-01f,8.274026e-02f,9.965711e-01f,
8.579731e-02f,9.963126e-01f,8.885355e-02f,9.960447e-01f,9.1908954e-02f,9.957674e-01f,9.49635e-02f,9.954808e-01f,
9.801714e-02f,9.951847e-01f,1.0106986e-01f,9.948793e-01f,1.0412163e-01f,9.945646e-01f,1.0717242e-01f,9.9424046e-01f,
1.10222206e-01f,9.93907e-01f,1.1327095e-01f,9.935641e-01f,1.1631863e-01f,9.932119e-01f,1.19365215e-01f,9.928504e-01f,
1.2241068e-01f,9.9247956e-01f,1.2545498e-01f,9.920993e-01f,1.284981e-01f,9.9170977e-01f,1.3154003e-01f,9.9131083e-01f,
1.345807e-01f,9.9090266e-01f,1.3762012e-01f,9.904851e-01f,1.4065824e-01f,9.900582e-01f,1.4369503e-01f,9.89622e-01f,
1.4673047e-01f,9.891765e-01f,1.4976454e-01f,9.8872167e-01f,1.5279719e-01f,9.882576e-01f,1.558284e-01f,9.8778415e-01f,
1.5885815e-01f,9.873014e-01f,1.618864e-01f,9.868094e-01f,1.6491312e-01f,9.863081e-01f,1.6793829e-01f,9.857975e-01f,
1.7096189e-01f,9.8527765e-01f,1.7398387e-01f,9.847485e-01f,1.7700422e-01f,9.842101e-01f,1.800229e-01f,9.836624e-01f,
1.8303989e-01f,9.831055e-01f,1.8605515e-01f,9.825393e-01f,1.8906866e-01f,9.819639e-01f,1.920804e-01f,9.813792e-01f,
1.9509032e-01f,9.8078525e-01f,1.980984e-01f,9.801821e-01f,2.0110464e-01f,9.795698e-01f,2.0410897e-01f,9.789482e-01f,
2.0711137e-01f,9.783174e-01f,2.1011184e-01f,9.7767735e-01f,2.1311031e-01f,9.7702813e-01f,2.161068e-01f,9.7636974e-01f,
2.1910124e-01f,9.757021e-01f,2.2209363e-01f,9.7502536e-01f,2.2508392e-01f,9.7433937e-01f,2.2807208e-01f,9.7364426e-01f,
2.310581e-01f,9.7293997e-01f,2.3404196e-01f,9.722265e-01f,2.370236e-01f,9.715039e-01f,2.4000302e-01f,9.7077215e-01f,
2.4298018e-01f,9.7003126e-01f,2.4595505e-01f,9.6928126e-01f,2.4892761e-01f,9.685221e-01f,2.518978e-01f,9.677538e-01f,
2.5486565e-01f,9.6697646e-01f,2.578311e-01f,9.6619e-01f,2.607941e-01f,9.6539444e-01f,2.6375467e-01f,9.645898e-01f,
2.6671275e-01f,9.6377605e-01f,2.696683e-01f,9.6295327e-01f,2.7262136e-01f,9.621214e-01f,2.7557182e-01f,9.6128047e-01f,
2.785197e-01f,9.604305e-01f,2.8146493e-01f,9.5957154e-01f,2.8440753e-01f,9.5870346e-01f,2.8734747e-01f,9.5782644e-01f,
2.9028466e-01f,9.5694035e-01f,2.9321915e-01f,9.5604527e-01f,2.961509e-01f,9.551412e-01f,2.9907984e-01f,9.542281e-01f,
3.0200595e-01f,9.53306e-01f,3.0492923e-01f,9.52375e-01f,3.0784965e-01f,9.51435e-01f,3.1076714e-01f,9.5048606e-01f,
3.1368175e-01f,9.4952816e-01f,3.1659338e-01f,9.485614e-01f,3.1950203e-01f,9.475856e-01f,3.224077e-01f,9.466009e-01f,
3.253103e-01f,9.456073e-01f,3.2820985e-01f,9.446048e-01f,3.311063e-01f,9.4359344e-01f,3.3399966e-01f,9.425732e-01f,
3.3688986e-01f,9.4154406e-01f,3.3977687e-01f,9.4050604e-01f,3.4266073e-01f,9.394592e-01f,3.4554133e-01f,9.3840355e-01f,
3.4841868e-01f,9.37339e-01f,3.5129276e-01f,9.3626565e-01f,3.5416353e-01f,9.351835e-01f,3.5703096e-01f,9.340925e-01f,
3.5989505e-01f,9.329928e-01f,3.6275572e-01f,9.318843e-01f,3.6561298e-01f,9.3076694e-01f,3.6846682e-01f,9.296409e-01f,
3.713172e-01f,9.285061e-01f,3.7416407e-01f,9.273625e-01f,3.7700742e-01f,9.262102e-01f,3.798472e-01f,9.2504925e-01f,
3.8268343e-01f,9.238795e-01f,3.8551605e-01f,9.227011e-01f,3.8834503e-01f,9.2151403e-01f,3.9117038e-01f,9.203183e-01f,
3.9399204e-01f,9.191139e-01f,3.9681e-01f,9.179008e-01f,3.996242e-01f,9.166791e-01f,4.0243465e-01f,9.154487e-01f,
4.052413e-01f,9.142098e-01f,4.0804416e-01f,9.129622e-01f,4.1084316e-01f,9.1170603e-01f,4.1363832e-01f,9.104413e-01f,
4.1642955e-01f,9.09168e-01f,4.192169e-01f,9.078861e-01f,4.2200026e-01f,9.065957e-01f,4.2477968e-01f,9.0529674e-01f,
4.2755508e-01f,9.039893e-01f,4.303265e-01f,9.026733e-01f,4.3309382e-01f,9.013488e-01f,4.358571e-01f,9.000159e-01f,
4.3861625e-01f,8.986745e-01f,4.4137126e-01f,8.9732456e-01f,4.4412214e-01f,8.9596623e-01f,4.4686884e-01f,8.945995e-01f,
4.4961134e-01f,8.932243e-01f,4.5234957e-01f,8.918407e-01f,4.5508358e-01f,8.9044875e-01f,4.578133e-01f,8.8904834e-01f,
4.6053872e-01f,8.8763964e-01f,4.632598e-01f,8.8622254e-01f,4.659765e-01f,8.847971e-01f,4.6868882e-01f,8.8336337e-01f,
4.7139674e-01f,8.819213e-01f,4.741002e-01f,8.804709e-01f,4.7679922e-01f,8.790122e-01f,4.7949377e-01f,8.775453e-01f,
4.8218378e-01f,8.760701e-01f,4.8486924e-01f,8.7458664e-01f,4.8755017e-01f,8.73095e-01f,4.9022648e-01f,8.715951e-01f,
4.928982e-01f,8.7008697e-01f,4.9556527e-01f,8.685707e-01f,4.9822766e-01f,8.6704624e-01f,5.0088537e-01f,8.655136e-01f,
5.0353837e-01f,8.6397284e-01f,5.0618666e-01f,8.6242396e-01f,5.0883013e-01f,8.6086696e-01f,5.114688e-01f,8.593018e-01f,
5.1410276e-01f,8.577286e-01f,5.167318e-01f,8.5614735e-01f,5.19356e-01f,8.54558e-01f,5.219753e-01f,8.529606e-01f,
5.2458966e-01f,8.513552e-01f,5.2719915e-01f,8.4974176e-01f,5.2980363e-01f,8.4812033e-01f,5.324031e-01f,8.464909e-01f,
5.3499764e-01f,8.448536e-01f,5.3758705e-01f,8.4320825e-01f,5.4017144e-01f,8.41555e-01f,5.427508e-01f,8.398938e-01f,
5.45325e-01f,8.382247e-01f,5.4789406e-01f,8.3654773e-01f,5.5045795e-01f,8.348629e-01f,5.530167e-01f,8.331702e-01f,
5.5557024e-01f,8.314696e-01f,5.581185e-01f,8.297612e-01f,5.6066155e-01f,8.2804507e-01f,5.6319934e-01f,8.2632107e-01f,
5.657318e-01f,8.245893e-01f,5.6825894e-01f,8.228498e-01f,5.7078075e-01f,8.211025e-01f,5.7329714e-01f,8.193475e-01f,
5.7580817e-01f,8.175848e-01f,5.7831377e-01f,8.1581444e-01f,5.8081394e-01f,8.140363e-01f,5.8330864e-01f,8.122506e-01f,
5.8579785e-01f,8.1045717e-01f,5.882816e-01f,8.0865616e-01f,5.907597e-01f,8.068476e-01f,5.932323e-01f,8.0503136e-01f,
5.956993e-01f,8.032075e-01f,5.981607e-01f,8.0137616e-01f,6.0061646e-01f,7.9953724e-01f,6.030666e-01f,7.9769087e-01f,
6.0551107e-01f,7.958369e-01f,6.079498e-01f,7.939755e-01f,6.103828e-01f,7.9210657e-01f,6.128101e-01f,7.902302e-01f,
6.152316e-01f,7.883464e-01f,6.176473e-01f,7.864552e-01f,6.200572e-01f,7.8455657e-01f,6.2246126e-01f,7.826506e-01f,
6.248595e-01f,7.807372e-01f,6.272518e-01f,7.788165e-01f,6.2963825e-01f,7.768885e-01f,6.3201874e-01f,7.749531e-01f,
6.343933e-01f,7.7301043e-01f,6.3676184e-01f,7.710605e-01f,6.3912445e-01f,7.6910335e-01f,6.4148104e-01f,7.671389e-01f,
6.4383155e-01f,7.6516724e-01f,6.4617604e-01f,7.631884e-01f,6.485144e-01f,7.612024e-01f,6.5084666e-01f,7.592092e-01f,
6.5317285e-01f,7.572088e-01f,6.5549284e-01f,7.552014e-01f,6.578067e-01f,7.531868e-01f,6.6011435e-01f,7.5116515e-01f,
6.624158e-01f,7.491364e-01f,6.64711e-01f,7.471006e-01f,6.6699994e-01f,7.4505776e-01f,6.692826e-01f,7.4300796e-01f,
6.71559e-01f,7.409511e-01f,6.73829e-01f,7.388873e-01f,6.760927e-01f,7.368166e-01f,6.7835003e-01f,7.347389e-01f,
6.80601e-01f,7.326543e-01f,6.8284553e-01f,7.3056275e-01f,6.850837e-01f,7.2846437e-01f,6.8731534e-01f,7.263591e-01f,
6.8954057e-01f,7.242471e-01f,6.917592e-01f,7.221282e-01f,6.9397146e-01f,7.2000253e-01f,6.961771e-01f,7.1787006e-01f,
6.9837624e-01f,7.1573085e-01f,7.005688e-01f,7.1358484e-01f,7.0275474e-01f,7.114322e-01f,7.0493406e-01f,7.092728e-01f,
7.0710677e-01f,7.0710677e-01f,7.092728e-01f,7.0493406e-01f,7.114322e-01f,7.0275474e-01f,7.1358484e-01f,7.005688e-01f,
7.1573085e-01f,6.9837624e-01f,7.1787006e-01f,6.961771e-01f,7.2000253e-01f,6.9397146e-01f,7.221282e-01f,6.917592e-01f,
7.242471e-01f,6.8954057e-01f,7.263591e-01f,6.8731534e-01f,7.2846437e-01f,6.850837e-01f,7.3056275e-01f,6.8284553e-01f,
7.326543e-01f,6.80601e-01f,7.347389e-01f,6.7835003e-01f,7.368166e-01f,6.760927e-01f,7.388873e-01f,6.73829e-01f,
7.409511e-01f,6.71559e-01f,7.4300796e-01f,6.692826e-01f,7.4505776e-01f,6.6699994e-01f,7.471006e-01f,6.64711e-01f,
7.491364e-01f,6.624158e-01f,7.5116515e-01f,6.6011435e-01f,7.531868e-01f,6.578067e-01f,7.552014e-01f,6.5549284e-01f,
7.572088e-01f,6.5317285e-01f,7.592092e-01f,6.5084666e-01f,7.612024e-01f,6.485144e-01f,7.631884e-01f,6.4617604e-01f,
7.6516724e-01f,6.4383155e-01f,7.671389e-01f,6.4148104e-01f,7.6910335e-01f,6.3912445e-01f,7.710605e-01f,6.3676184e-01f,
7.7301043e-01f,6.343933e-01f,7.749531e-01f,6.3201874e-01f,7.768885e-01f,6.2963825e-01f,7.788165e-01f,6.272518e-01f,
7.807372e-01f,6.248595e-01f,7.826506e-01f,6.2246126e-01f,7.8455657e-01f,6.200572e-01f,7.864552e-01f,6.176473e-01f,
7.883464e-01f,6.152316e-01f,7.902302e-01f,6.128101e-01f,7.9210657e-01f,6.103828e-01f,7.939755e-01f,6.079498e-01f,
7.958369e-01f,6.0551107e-01f,7.9769087e-01f,6.030666e-01f,7.9953724e-01f,6.0061646e-01f,8.0137616e-01f,5.981607e-01f,
8.032075e-01f,5.956993e-01f,8.0503136e-01f,5.932323e-01f,8.068476e-01f,5.907597e-01f,8.0865616e-01f,5.882816e-01f,
8.1045717e-01f,5.8579785e-01f,8.122506e-01f,5.8330864e-01f,8.140363e-01f,5.8081394e-01f,8.1581444e-01f,5.7831377e-01f,
8.175848e-01f,5.7580817e-01f,8.193475e-01f,5.7329714e-01f,8.211025e-01f,5.7078075e-01f,8.228498e-01f,5.6825894e-01f,
8.245893e-01f,5.657318e-01f,8.2632107e-01f,5.6319934e-01f,8.2804507e-01f,5.6066155e-01f,8.297612e-01f,5.581185e-01f,
8.314696e-01f,5.5557024e-01f,8.331702e-01f,5.530167e-01f,8.348629e-01f,5.5045795e-01f,8.3654773e-01f,5.4789406e-01f,
8.382247e-01f,5.45325e-01f,8.398938e-01f,5.427508e-01f,8.41555e-01f,5.4017144e-01f,8.4320825e-01f,5.3758705e-01f,
8.448536e-01f,5.3499764e-01f,8.464909e-01f,5.324031e-01f,8.4812033e-01f,5.2980363e-01f,8.4974176e-01f,5.2719915e-01f,
8.513552e-01f,5.2458966e-01f,8.529606e-01f,5.219753e-01f,8.54558e-01f,5.19356e-01f,8.5614735e-01f,5.167318e-01f,
8.577286e-01f,5.1410276e-01f,8.593018e-01f,5.114688e-01f,8.6086696e-01f,5.0883013e-01f,8.6242396e-01f,5.0618666e-01f,
8.6397284e-01f,5.0353837e-01f,8.655136e-01f,5.0088537e-01f,8.6704624e-01f,4.9822766e-01f,8.685707e-01f,4.9556527e-01f,
8.7008697e-01f,4.928982e-01f,8.715951e-01f,4.9022648e-01f,8.73095e-01f,4.8755017e-01f,8.7458664e-01f,4.8486924e-01f,
8.760701e-01f,4.8218378e-01f,8.775453e-01f,4.7949377e-01f,8.790122e-01f,4.7679922e-01f,8.804709e-01f,4.741002e-01f,
8.819213e-01f,4.7139674e-01f,8.8336337e-01f,4.6868882e-01f,8.847971e-01f,4.659765e-01f,8.8622254e-01f,4.632598e-01f,
8.8763964e-01f,4.6053872e-01f,8.8904834e-01f,4.578133e-01f,8.9044875e-01f,4.5508358e-01f,8.918407e-01f,4.5234957e-01f,
8.932243e-01f,4.4961134e-01f,8.945995e-01f,4.4686884e-01f,8.9596623e-01f,4.4412214e-01f,8.9732456e-01f,4.4137126e-01f,
8.986745e-01f,4.3861625e-01f,9.000159e-01f,4.358571e-01f,9.013488e-01f,4.3309382e-01f,9.026733e-01f,4.303265e-01f,
9.039893e-01f,4.2755508e-01f,9.0529674e-01f,4.2477968e-01f,9.065957e-01f,4.2200026e-01f,9.078861e-01f,4.192169e-01f,
9.09168e-01f,4.1642955e-01f,9.104413e-01f,4.1363832e-01f,9.1170603e-01f,4.1084316e-01f,9.129622e-01f,4.0804416e-01f,
9.142098e-01f,4.052413e-01f,9.154487e-01f,4.0243465e-01f,9.166791e-01f,3.996242e-01f,9.179008e-01f,3.9681e-01f,
9.191139e-01f,3.9399204e-01f,9.203183e-01f,3.9117038e-01f,9.2151403e-01f,3.8834503e-01f,9.227011e-01f,3.8551605e-01f,
9.238795e-01f,3.8268343e-01f,9.2504925e-01f,3.798472e-01f,9.262102e-01f,3.7700742e-01f,9.273625e-01f,3.7416407e-01f,
9.285061e-01f,3.713172e-01f,9.296409e-01f,3.6846682e-01f,9.3076694e-01f,3.6561298e-01f,9.318843e-01f,3.6275572e-01f,
9.329928e-01f,3.5989505e-01f,9.340925e-01f,3.5703096e-01f,9.351835e-01f,3.5416353e-01f,9.3626565e-01f,3.5129276e-01f,
9.37339e-01f,3.4841868e-01f,9.3840355e-01f,3.4554133e-01f,9.394592e-01f,3.4266073e-01f,9.4050604e-01f,3.3977687e-01f,
9.4154406e-01f,3.3688986e-01f,9.425732e-01f,3.3399966e-01f,9.4359344e-01f,3.311063e-01f,9.446048e-01f,3.2820985e-01f,
9.456073e-01f,3.253103e-01f,9.466009e-01f,3.224077e-01f,9.475856e-01f,3.1950203e-01f,9.485614e-01f,3.1659338e-01f,
9.4952816e-01f,3.1368175e-01f,9.5048606e-01f,3.1076714e-01f,9.51435e-01f,3.0784965e-01f,9.52375e-01f,3.0492923e-01f,
9.53306e-01f,3.0200595e-01f,9.542281e-01f,2.9907984e-01f,9.551412e-01f,2.961509e-01f,9.5604527e-01f,2.9321915e-01f,
9.5694035e-01f,2.9028466e-01f,9.5782644e-01f,2.8734747e-01f,9.5870346e-01f,2.8440753e-01f,9.5957154e-01f,2.8146493e-01f,
9.604305e-01f,2.785197e-01f,9.6128047e-01f,2.7557182e-01f,9.621214e-01f,2.7262136e-01f,9.6295327e-01f,2.696683e-01f,
9.6377605e-01f,2.6671275e-01f,9.645898e-01f,2.6375467e-01f,9.6539444e-01f,2.607941e-01f,9.6619e-01f,2.578311e-01f,
9.6697646e-01f,2.5486565e-01f,9.677538e-01f,2.518978e-01f,9.685221e-01f,2.4892761e-01f,9.6928126e-01f,2.4595505e-01f,
9.7003126e-01f,2.4298018e-01f,9.7077215e-01f,2.4000302e-01f,9.715039e-01f,2.370236e-01f,9.722265e-01f,2.3404196e-01f,
9.7293997e-01f,2.310581e-01f,9.7364426e-01f,2.2807208e-01f,9.7433937e-01f,2.2508392e-01f,9.7502536e-01f,2.2209363e-01f,
9.757021e-01f,2.1910124e-01f,9.7636974e-01f,2.161068e-01f,9.7702813e-01f,2.1311031e-01f,9.7767735e-01f,2.1011184e-01f,
9.783174e-01f,2.0711137e-01f,9.789482e-01f,2.0410897e-01f,9.795698e-01f,2.0110464e-01f,9.801821e-01f,1.980984e-01f,
9.8078525e-01f,1.9509032e-01f,9.813792e-01f,1.920804e-01f,9.819639e-01f,1.8906866e-01f,9.825393e-01f,1.8605515e-01f,
9.831055e-01f,1.8303989e-01f,9.836624e-01f,1.800229e-01f,9.842101e-01f,1.7700422e-01f,9.847485e-01f,1.7398387e-01f,
9.8527765e-01f,1.7096189e-01f,9.857975e-01f,1.6793829e-01f,9.863081e-01f,1.6491312e-01f,9.868094e-01f,1.618864e-01f,
9.873014e-01f,1.5885815e-01f,9.8778415e-01f,1.558284e-01f,9.882576e-01f,1.5279719e-01f,9.8872167e-01f,1.4976454e-01f,
9.891765e-01f,1.4673047e-01f,9.89622e-01f,1.4369503e-01f,9.900582e-01f,1.4065824e-01f,9.904851e-01f,1.3762012e-01f,
9.9090266e-01f,1.345807e-01f,9.9131083e-01f,1.3154003e-01f,9.9170977e-01f,1.284981e-01f,9.920993e-01f,1.2545498e-01f,
9.9247956e-01f,1.2241068e-01f,9.928504e-01f,1.19365215e-01f,9.932119e-01f,1.1631863e-01f,9.935641e-01f,1.1327095e-01f,
9.93907e-01f,1.10222206e-01f,9.9424046e-01f,1.0717242e-01f,9.945646e-01f,1.0412163e-01f,9.948793e-01f,1.0106986e-01f,
9.951847e-01f,9.801714e-02f,9.954808e-01f,9.49635e-02f,9.957674e-01f,9.1908954e-02f,9.960447e-01f,8.885355e-02f,
9.963126e-01f,8.579731e-02f,9.965711e-01f,8.274026e-02f,9.968203e-01f,7.968244e-02f,9.9706006e-01f,7.6623864e-02f,
9.9729043e-01f,7.356457e-02f,9.9751145e-01f,7.0504576e-02f,9.9772304e-01f,6.744392e-02f,9.979253e-01f,6.438263e-02f,
9.981181e-01f,6.1320737e-02f,9.9830157e-01f,5.8258265e-02f,9.9847555e-01f,5.5195246e-02f,9.9864024e-01f,5.2131705e-02f,
9.9879545e-01f,4.9067676e-02f,9.989413e-01f,4.600318e-02f,9.9907774e-01f,4.293826e-02f,9.9920475e-01f,3.9872926e-02f,
9.9932235e-01f,3.6807224e-02f,9.994306e-01f,3.3741172e-02f,9.995294e-01f,3.0674804e-02f,9.996188e-01f,2.7608145e-02f,
9.996988e-01f,2.4541229e-02f,9.997694e-01f,2.147408e-02f,9.998306e-01f,1.840673e-02f,9.9988234e-01f,1.5339206e-02f,
9.999247e-01f,1.2271538e-02f,9.999576e-01f,9.203754e-03f,9.9998116e-01f,6.1358847e-03f,9.999953e-01f,3.0679568e-03f,
};


__device__ __forceinline__ float bfr(float f) { return (float)(__bf16)f; }
__device__ __forceinline__ float bf2f(unsigned short u) { return __uint_as_float(((unsigned)u) << 16); }
__device__ __forceinline__ unsigned pack_bf16(float lo, float hi) {
    typedef __bf16 v2bf __attribute__((ext_vector_type(2)));
    v2f p = {lo, hi};
    v2bf b = __builtin_convertvector(p, v2bf);
    return __builtin_bit_cast(unsigned, b);
}
__device__ __forceinline__ v2f tw640(int m) { return *(const v2f*)(TW640 + 2 * m); }
__device__ __forceinline__ v2f tw2048(int m) { return *(const v2f*)(TW2048 + 2 * m); }

template <int S>
__device__ __forceinline__ v2f padd(v2f a, v2f b) {
    v2f d;
    if (S < 0)
        asm("v_pk_add_f32 %0, %1, %2 op_sel:[0,1] op_sel_hi:[1,0] neg_hi:[0,1]" : "=v"(d) : "v"(a), "v"(b));
    else
        asm("v_pk_add_f32 %0, %1, %2 op_sel:[0,1] op_sel_hi:[1,0] neg_lo:[0,1]" : "=v"(d) : "v"(a), "v"(b));
    return d;
}
template <int S>
__device__ __forceinline__ v2f psub(v2f a, v2f b) { return padd<-S>(a, b); }

template <int S>
__device__ __forceinline__ v2f twmul(v2f a, v2f w) {
    v2f t, d;
    if (S < 0) {
        asm("v_pk_mul_f32 %0, %1, %2 op_sel_hi:[0,1]" : "=v"(t) : "v"(a), "v"(w));
        asm("v_pk_fma_f32 %0, %1, %2, %3 op_sel:[1,1,0] op_sel_hi:[1,0,1] neg_lo:[0,1,0]"
            : "=v"(d) : "v"(a), "v"(w), "v"(t));
    } else {
        asm("v_pk_mul_f32 %0, %1, %2 op_sel_hi:[0,1] neg_hi:[0,1]" : "=v"(t) : "v"(a), "v"(w));
        asm("v_pk_fma_f32 %0, %1, %2, %3 op_sel:[1,1,0] op_sel_hi:[1,0,1]" : "=v"(d) : "v"(a), "v"(w), "v"(t));
    }
    return d;
}
__device__ __forceinline__ v2f vfma(v2f a, float c, v2f e) {
    v2f cc = {c, c};
    return __builtin_elementwise_fma(a, cc, e);
}

template <int NP>
__device__ __forceinline__ void tw_powers(v2f w1, v2f* w) {
    w[0] = w1;
    w[1] = twmul<-1>(w1, w1);
    w[2] = twmul<-1>(w[1], w1);
    if (NP >= 4) w[3] = twmul<-1>(w[1], w[1]);
    if (NP >= 7) {
        w[4] = twmul<-1>(w[3], w1);
        w[5] = twmul<-1>(w[2], w[2]);
        w[6] = twmul<-1>(w[3], w[2]);
    }
    if (NP >= 9) {
        w[7] = twmul<-1>(w[3], w[3]);
        w[8] = twmul<-1>(w[7], w1);
    }
}

template <int S>
__device__ __forceinline__ void dft8(v2f* v) {
    const float c = 0.70710678118654752f;
    v2f a0 = v[0] + v[4], a1 = v[0] - v[4], a2 = v[2] + v[6], a3 = v[2] - v[6];
    v2f a4 = v[1] + v[5], a5 = v[1] - v[5], a6 = v[3] + v[7], a7 = v[3] - v[7];
    v2f E0 = a0 + a2, E2 = a0 - a2, E1 = padd<S>(a1, a3), E3 = psub<S>(a1, a3);
    v2f b0 = a4 + a6, b2 = a4 - a6, p = padd<S>(a5, a7), q = psub<S>(a5, a7);
    v2f r1 = padd<S>(p, p), r3 = psub<S>(q, q);
    v[0] = E0 + b0;
    v[4] = E0 - b0;
    v[2] = padd<S>(E2, b2);
    v[6] = psub<S>(E2, b2);
    v[1] = vfma(r1, c, E1);
    v[5] = vfma(r1, -c, E1);
    v[3] = vfma(r3, -c, E3);
    v[7] = vfma(r3, c, E3);
}

template <int S>
__device__ __forceinline__ void dft4(v2f& v0, v2f& v1, v2f& v2, v2f& v3) {
    v2f d0 = v0 + v2, d1 = v0 - v2, d2 = v1 + v3, d3 = v1 - v3;
    v0 = d0 + d2;
    v2 = d0 - d2;
    v1 = padd<S>(d1, d3);
    v3 = psub<S>(d1, d3);
}

template <int S>
__device__ __forceinline__ void dft5(v2f v0, v2f v1, v2f v2, v2f v3, v2f v4, v2f* o) {
    const float c1 = 0.30901699437494742f, c2 = -0.80901699437494742f;
    const float s1 = 0.95105651629515357f, s2 = 0.58778525229247313f;
    v2f t1 = v1 + v4, t2 = v2 + v3, t3 = v1 - v4, t4 = v2 - v3;
    v2f a1 = vfma(t2, c2, vfma(t1, c1, v0));
    v2f a2 = vfma(t2, c1, vfma(t1, c2, v0));
    v2f b1 = vfma(t4, s2, t3 * s1);
    v2f b2 = vfma(t4, -s1, t3 * s2);
    o[0] = v0 + t1 + t2;
    o[1] = padd<S>(a1, b1);
    o[4] = psub<S>(a1, b1);
    o[2] = padd<S>(a2, b2);
    o[3] = psub<S>(a2, b2);
}

template <int S>
__device__ __forceinline__ void dft10(v2f* v) {
    v2f g0[5], g1[5];
    dft5<S>(v[0], v[2], v[4], v[6], v[8], g0);
    dft5<S>(v[5], v[7], v[9], v[1], v[3], g1);
#pragma unroll
    for (int k2 = 0; k2 < 5; ++k2) {
        v[(6 * k2) % 10] = g0[k2] + g1[k2];
        v[(5 + 6 * k2) % 10] = g0[k2] - g1[k2];
    }
}

template <int CTRL>
__device__ __forceinline__ float dpp_addc(float x) {
    int r = __builtin_amdgcn_update_dpp(0, __float_as_int(x), CTRL, 0xF, 0xF, true);
    return x + __int_as_float(r);
}
__device__ __forceinline__ float wave_sum(float x) {
    x = dpp_addc<0xB1>(x);
    x = dpp_addc<0x4E>(x);
    x = dpp_addc<0x124>(x);
    x = dpp_addc<0x128>(x);
    int xi = __float_as_int(x);
    return (__int_as_float(__builtin_amdgcn_readlane(xi, 0)) + __int_as_float(__builtin_amdgcn_readlane(xi, 16))) +
           (__int_as_float(__builtin_amdgcn_readlane(xi, 32)) + __int_as_float(__builtin_amdgcn_readlane(xi, 48)));
}

__device__ __forceinline__ void ln2(float (&a)[10], float (&b)[10]) {
    float sa = 0.f, sb = 0.f;
#pragma unroll
    for (int i = 0; i < 10; ++i) { sa += a[i]; sb += b[i]; }
    sa = wave_sum(sa) * (1.0f / ND);
    sb = wave_sum(sb) * (1.0f / ND);
    float qa = 0.f, qb = 0.f;
#pragma unroll
    for (int i = 0; i < 10; ++i) {
        float da = a[i] - sa, db = b[i] - sb;
        qa = fmaf(da, da, qa);
        qb = fmaf(db, db, qb);
    }
    qa = wave_sum(qa) * (1.0f / ND);
    qb = wave_sum(qb) * (1.0f / ND);
    const float ma = bfr(sa), mb = bfr(sb);
    const float da = bfr(sqrtf(bfr(bfr(qa) + 1e-5f)));
    const float db = bfr(sqrtf(bfr(bfr(qb) + 1e-5f)));
    const float ra = __builtin_amdgcn_rcpf(da), rb = __builtin_amdgcn_rcpf(db);
#pragma unroll
    for (int i = 0; i < 10; ++i) {
        const float na = bfr(a[i] - ma), nb = bfr(b[i] - mb);
        float q1 = na * ra, q2 = nb * rb;
        q1 = fmaf(fmaf(-q1, da, na), ra, q1);
        q2 = fmaf(fmaf(-q2, db, nb), rb, q2);
        a[i] = bfr(q1);
        b[i] = bfr(q2);
    }
}

__device__ __forceinline__ void transpose_tile_wave(const unsigned short* __restrict__ src,
                                                    unsigned short* __restrict__ dst, int R, int C, int tr, int tc,
                                                    int lane) {
    const int lc = lane & 7, lr = lane >> 3;
    uint4 in[8];
#pragma unroll
    for (int i = 0; i < 8; ++i)
        in[i] = *(const uint4*)(src + (size_t)(tr * 64 + lr * 8 + i) * C + tc * 64 + lc * 8);
    unsigned P[4][4][2];
#pragma unroll
    for (int i2 = 0; i2 < 4; ++i2) {
        const unsigned* A = (const unsigned*)&in[2 * i2];
        const unsigned* B = (const unsigned*)&in[2 * i2 + 1];
#pragma unroll
        for (int t = 0; t < 4; ++t) {
            P[i2][t][0] = __builtin_amdgcn_perm(B[t], A[t], 0x05040100u);
            P[i2][t][1] = __builtin_amdgcn_perm(B[t], A[t], 0x07060302u);
        }
    }
#pragma unroll
    for (int c = 0; c < 8; ++c) {
        uint4 o = make_uint4(P[0][c >> 1][c & 1], P[1][c >> 1][c & 1], P[2][c >> 1][c & 1], P[3][c >> 1][c & 1]);
        *(uint4*)(dst + (size_t)(tc * 64 + lc * 8 + c) * R + tr * 64 + lr * 8) = o;
    }
}

__device__ __forceinline__ void filter_tile_wave(const unsigned short* __restrict__ wr,
                                                 const unsigned short* __restrict__ wi, unsigned* __restrict__ Wt,
                                                 unsigned* lds, int ts, int tk, int lane) {
    const int k0 = tk * 64, s0 = ts * 32;
    const int kl = min(k0 + lane, NKF - 1);
    unsigned v[32];
#pragma unroll
    for (int i = 0; i < 32; ++i) {
        const size_t idx = (size_t)(s0 + i) * NKF + kl;
        v[i] = (unsigned)wr[idx] | ((unsigned)wi[idx] << 16);
    }
#pragma unroll
    for (int i = 0; i < 32; ++i) lds[i * 65 + lane] = v[i];
    const int kq = lane >> 3, sq = lane & 7;
#pragma unroll
    for (int j = 0; j < 8; ++j) {
        const int k = 8 * j + kq;
        u32x4 o;
#pragma unroll
        for (int e = 0; e < 4; ++e) o[e] = lds[(4 * sq + e) * 65 + k];
        if (k0 + k < NKF) *(u32x4*)(Wt + (size_t)(k0 + k) * NS + s0 + 4 * sq) = o;
    }
}

#define S1_FFT_BLOCKS (NS / 8)
#define TR_TILES (2 * (ND / 64) * (NF / 64))
#define TR_BLOCKS ((TR_TILES + 4) / 5)
#define FT_TILES ((NS / 32) * ((NKF + 63) / 64))
#define FT_BLOCKS ((FT_TILES + 3) / 4)
#define FS 648

__global__ __launch_bounds__(320) void k_stage1(const unsigned short* __restrict__ x, v2f* __restrict__ spec,
                                                const unsigned short* __restrict__ wr,
                                                const unsigned short* __restrict__ wi, unsigned* __restrict__ Wt) {
    __shared__ __attribute__((aligned(16))) v2f bufA[4 * FS];
    __shared__ __attribute__((aligned(16))) v2f bufB[4 * FS];
    const int tid = threadIdx.x, wave = tid >> 6, lane = tid & 63;
    int bid = blockIdx.x;
    if (bid < FT_BLOCKS) {
        const int t = bid * 4 + wave;
        if (wave < 4 && t < FT_TILES)
            filter_tile_wave(wr, wi, Wt, (unsigned*)bufA + wave * (32 * 65), t / 6, t % 6, lane);
        return;
    }
    bid -= FT_BLOCKS;
    const int s0 = bid * 8;
    const int f = tid / 80, j = tid - f * 80;
    const v2f w2b = tw640((j % 10) * 8), w3b = tw640(j);
    if (wave < 4) {
        const unsigned short* ra = x + (size_t)(s0 + 2 * wave) * ND;
        float a[10], b[10];
#pragma unroll
        for (int i = 0; i < 10; ++i) {
            a[i] = bf2f(ra[lane + 64 * i]);
            b[i] = bf2f(ra[ND + lane + 64 * i]);
        }
        ln2(a, b);
        v2f v[10];
#pragma unroll
        for (int i = 0; i < 10; ++i) v[i] = v2f{a[i], b[i]};
        dft10<-1>(v);
#pragma unroll
        for (int r = 0; r < 10; ++r) bufA[wave * FS + 10 * lane + r] = v[r];
    }
    __syncthreads();
    {
        v2f v[8];
#pragma unroll
        for (int r = 0; r < 8; ++r) v[r] = bufA[f * FS + j + 80 * r];
        v2f w2v[7];
        tw_powers<7>(w2b, w2v);
#pragma unroll
        for (int r = 1; r < 8; ++r) v[r] = twmul<-1>(v[r], w2v[r - 1]);
        dft8<-1>(v);
        const int o = f * FS + (j / 10) * 80 + j % 10;
#pragma unroll
        for (int r = 0; r < 8; ++r) bufB[o + 10 * r] = v[r];
    }
    __syncthreads();
    {
        v2f v[8];
#pragma unroll
        for (int r = 0; r < 8; ++r) v[r] = bufB[f * FS + j + 80 * r];
        v2f w3v[7];
        tw_powers<7>(w3b, w3v);
#pragma unroll
        for (int r = 1; r < 8; ++r) v[r] = twmul<-1>(v[r], w3v[r - 1]);
        dft8<-1>(v);
#pragma unroll
        for (int r = 0; r < 8; ++r) bufA[f * FS + j + 80 * r] = v[r];
    }
    __syncthreads();
#pragma unroll
    for (int it = 0; it < 5; ++it) {
        const int i = tid + 320 * it;
        if (it < 4 || tid < 4) {
            const int k = i >> 2, p = i & 3;
            const v2f zk = bufA[p * FS + k];
            const v2f zn = bufA[p * FS + ((ND - k) % ND)];
            float4 o;
            o.x = 0.5f * (zk.x + zn.x);
            o.y = 0.5f * (zk.y - zn.y);
            o.z = 0.5f * (zk.y + zn.y);
            o.w = -0.5f * (zk.x - zn.x);
            *(float4*)(spec + (size_t)k * NS + s0 + 2 * p) = o;
        }
    }
}

#define PX1(i) ((i) + ((i) >> 3))
#define PX2(i) ((i) + (((i) >> 5) << 2))
#define S2_BUF 2304

#define TR2_BLOCKS ((TR_TILES / 2 + 3) / 4)
__global__ __launch_bounds__(256) void k_stage2(v2f* __restrict__ spec, const unsigned* __restrict__ Wt,
                                                const unsigned short* __restrict__ w1,
                                                unsigned short* __restrict__ w1t) {
    __shared__ __attribute__((aligned(16))) v2f bA[S2_BUF];
    __shared__ __attribute__((aligned(16))) v2f bB[S2_BUF];
    const int tid = threadIdx.x;
    if (blockIdx.x < TR2_BLOCKS) {
        const int t = blockIdx.x * 4 + (tid >> 6);
        if (t < TR_TILES / 2) transpose_tile_wave(w1, w1t, ND, NF, t / (NF / 64), t % (NF / 64), tid & 63);
        return;
    }
    const int orig = blockIdx.x - TR2_BLOCKS;
    const int q = NKF / 8, r8 = NKF % 8, xcd = orig % 8;
    const int k = (xcd < r8 ? xcd * (q + 1) : r8 * (q + 1) + (xcd - r8) * q) + orig / 8;
    v2f* col = spec + (size_t)k * NS;

    v2f v[8];
#pragma unroll
    for (int m = 0; m < 8; ++m) v[m] = col[tid + 256 * m];
    unsigned wf[8];
#pragma unroll
    for (int m = 0; m < 8; ++m) wf[m] = Wt[(size_t)k * NS + tid + 256 * m];
    const v2f w2b = tw2048((tid & 7) * 32), w3b = tw2048((tid & 63) * 4);
    const v2f w4b0 = tw2048(tid), w4b1 = tw2048(tid + 256);
    v2f w2v[7], w3v[7], w4v[6];
    tw_powers<7>(w2b, w2v);
    tw_powers<7>(w3b, w3v);
    tw_powers<3>(w4b0, w4v);
    tw_powers<3>(w4b1, w4v + 3);

    const int o2 = (tid >> 3) * 64 + (tid & 7);
    const int o3 = (tid >> 6) * 512 + (tid & 63);

    dft8<-1>(v);
#pragma unroll
    for (int r = 0; r < 8; ++r) bA[PX1(8 * tid + r)] = v[r];
    __syncthreads();
#pragma unroll
    for (int r = 0; r < 8; ++r) v[r] = bA[PX1(tid + 256 * r)];
#pragma unroll
    for (int r = 1; r < 8; ++r) v[r] = twmul<-1>(v[r], w2v[r - 1]);
    dft8<-1>(v);
#pragma unroll
    for (int r = 0; r < 8; ++r) bB[PX2(o2 + 8 * r)] = v[r];
    __syncthreads();
#pragma unroll
    for (int r = 0; r < 8; ++r) v[r] = bB[PX2(tid + 256 * r)];
#pragma unroll
    for (int r = 1; r < 8; ++r) v[r] = twmul<-1>(v[r], w3v[r - 1]);
    dft8<-1>(v);
#pragma unroll
    for (int r = 0; r < 8; ++r) bA[o3 + 64 * r] = v[r];
    __syncthreads();
#pragma unroll
    for (int b = 0; b < 2; ++b) {
        v2f u0 = bA[tid + 256 * b], u1 = bA[tid + 256 * b + 512], u2 = bA[tid + 256 * b + 1024],
            u3 = bA[tid + 256 * b + 1536];
        u1 = twmul<-1>(u1, w4v[b * 3 + 0]);
        u2 = twmul<-1>(u2, w4v[b * 3 + 1]);
        u3 = twmul<-1>(u3, w4v[b * 3 + 2]);
        dft4<-1>(u0, u1, u2, u3);
        v[b] = u0; v[b + 2] = u1; v[b + 4] = u2; v[b + 6] = u3;
    }
#pragma unroll
    for (int m = 0; m < 8; ++m) {
        v2f w = {__uint_as_float(wf[m] << 16), __uint_as_float(wf[m] & 0xffff0000u)};
        v[m] = twmul<-1>(v[m], w);
    }
    dft8<1>(v);
#pragma unroll
    for (int r = 0; r < 8; ++r) bB[PX1(8 * tid + r)] = v[r];
    __syncthreads();
#pragma unroll
    for (int r = 0; r < 8; ++r) v[r] = bB[PX1(tid + 256 * r)];
#pragma unroll
    for (int r = 1; r < 8; ++r) v[r] = twmul<1>(v[r], w2v[r - 1]);
    dft8<1>(v);
#pragma unroll
    for (int r = 0; r < 8; ++r) bA[PX2(o2 + 8 * r)] = v[r];
    __syncthreads();
#pragma unroll
    for (int r = 0; r < 8; ++r) v[r] = bA[PX2(tid + 256 * r)];
#pragma unroll
    for (int r = 1; r < 8; ++r) v[r] = twmul<1>(v[r], w3v[r - 1]);
    dft8<1>(v);
#pragma unroll
    for (int r = 0; r < 8; ++r) bB[o3 + 64 * r] = v[r];
    __syncthreads();
    const float scale = 1.0f / ((float)NS * (float)ND);
#pragma unroll
    for (int b = 0; b < 2; ++b) {
        v2f u0 = bB[tid + 256 * b], u1 = bB[tid + 256 * b + 512], u2 = bB[tid + 256 * b + 1024],
            u3 = bB[tid + 256 * b + 1536];
        u1 = twmul<1>(u1, w4v[b * 3 + 0]);
        u2 = twmul<1>(u2, w4v[b * 3 + 1]);
        u3 = twmul<1>(u3, w4v[b * 3 + 2]);
        dft4<1>(u0, u1, u2, u3);
        col[tid + 256 * b] = u0 * scale;
        col[tid + 256 * b + 512] = u1 * scale;
        col[tid + 256 * b + 1024] = u2 * scale;
        col[tid + 256 * b + 1536] = u3 * scale;
    }
}

#define TR3_BLOCKS ((TR_TILES / 2 + 4) / 5)
__global__ __launch_bounds__(320) void k_stage3(const v2f* __restrict__ spec, const unsigned short* __restrict__ x,
                                                unsigned short* __restrict__ hbuf, unsigned short* __restrict__ hn,
                                                const unsigned short* __restrict__ w2,
                                                unsigned short* __restrict__ w2t) {
    __shared__ __attribute__((aligned(16))) v2f bufA[4 * FS];
    __shared__ __attribute__((aligned(16))) v2f bufB[4 * FS];
    const int tid = threadIdx.x, wave = tid >> 6, lane = tid & 63;
    if (blockIdx.x < TR3_BLOCKS) {
        const int u = blockIdx.x * 5 + wave;
        if (u < TR_TILES / 2) transpose_tile_wave(w2, w2t, NF, ND, u / (ND / 64), u % (ND / 64), lane);
        return;
    }
    const int s0 = (blockIdx.x - TR3_BLOCKS) * 8;
    const int f = tid / 80, j = tid - f * 80;
    float4 q[5];
#pragma unroll
    for (int it = 0; it < 5; ++it) {
        const int i = min(tid + 320 * it, NKF * 4 - 1);
        q[it] = *(const float4*)(spec + (size_t)(i >> 2) * NS + s0 + 2 * (i & 3));
    }
    const v2f w2b = tw640((j & 7) * 10), w3b = tw640(lane);
    unsigned short xa[10], xb[10];
    if (wave < 4) {
        const unsigned short* ra = x + (size_t)(s0 + 2 * wave) * ND;
#pragma unroll
        for (int i = 0; i < 10; ++i) {
            xa[i] = ra[lane + 64 * i];
            xb[i] = ra[ND + lane + 64 * i];
        }
    }
#pragma unroll
    for (int it = 0; it < 5; ++it) {
        const int i = tid + 320 * it;
        if (it < 4 || tid < 4) {
            const int k = i >> 2, p = i & 3;
            const bool edge = (k == 0) || (k == ND / 2);
            const float yai = edge ? 0.f : q[it].y, ybi = edge ? 0.f : q[it].w;
            bufA[p * FS + k] = v2f{q[it].x - ybi, yai + q[it].z};
            if (!edge) bufA[p * FS + ND - k] = v2f{q[it].x + ybi, q[it].z - yai};
        }
    }
    __syncthreads();
    {
        v2f v[8];
#pragma unroll
        for (int r = 0; r < 8; ++r) v[r] = bufA[f * FS + j + 80 * r];
        dft8<1>(v);
#pragma unroll
        for (int r = 0; r < 8; ++r) bufB[f * FS + 8 * j + r] = v[r];
    }
    __syncthreads();
    {
        v2f v[8];
#pragma unroll
        for (int r = 0; r < 8; ++r) v[r] = bufB[f * FS + j + 80 * r];
        v2f w2v[7];
        tw_powers<7>(w2b, w2v);
#pragma unroll
        for (int r = 1; r < 8; ++r) v[r] = twmul<1>(v[r], w2v[r - 1]);
        dft8<1>(v);
        const int o = f * FS + (j >> 3) * 64 + (j & 7);
#pragma unroll
        for (int r = 0; r < 8; ++r) bufA[o + 8 * r] = v[r];
    }
    __syncthreads();
    if (wave < 4) {
        v2f v[10];
#pragma unroll
        for (int r = 0; r < 10; ++r) v[r] = bufA[wave * FS + lane + 64 * r];
        v2f w3v[9];
        tw_powers<9>(w3b, w3v);
#pragma unroll
        for (int r = 1; r < 10; ++r) v[r] = twmul<1>(v[r], w3v[r - 1]);
        dft10<1>(v);
        float ha[10], hb2[10];
#pragma unroll
        for (int i = 0; i < 10; ++i) {
            ha[i] = bfr(bf2f(xa[i]) + bfr(v[i].x));
            hb2[i] = bfr(bf2f(xb[i]) + bfr(v[i].y));
        }
        unsigned short* ho = hbuf + (size_t)(s0 + 2 * wave) * ND;
#pragma unroll
        for (int i = 0; i < 10; ++i) {
            ho[lane + 64 * i] = (unsigned short)(__float_as_uint(ha[i]) >> 16);
            ho[ND + lane + 64 * i] = (unsigned short)(__float_as_uint(hb2[i]) >> 16);
        }
        ln2(ha, hb2);
        ln2(ha, hb2);
        unsigned short* no = hn + (size_t)(s0 + 2 * wave) * ND;
#pragma unroll
        for (int i = 0; i < 10; ++i) {
            no[lane + 64 * i] = (unsigned short)(__float_as_uint(ha[i]) >> 16);
            no[ND + lane + 64 * i] = (unsigned short)(__float_as_uint(hb2[i]) >> 16);
        }
    }
}

template <int BM, int BN, int KD, int WM, int WN, int P, int DIRECT_A, int EPI>
__global__ __launch_bounds__(WM* WN * 64) void k_gemm(const unsigned short* __restrict__ A,
                                                      const unsigned short* __restrict__ Bt, int N,
                                                      const unsigned short* __restrict__ bias,
                                                      const unsigned short* __restrict__ hres,
                                                      unsigned short* __restrict__ outp) {
    constexpr int NW = WM * WN, NTHR = NW * 64;
    constexpr int WTM = BM / WM, WTN = BN / WN, MT = WTM / 16, NT = WTN / 16;
    constexpr int AROWS = DIRECT_A ? 0 : BM;
    constexpr int ROWS = AROWS + BN, SLOTS = ROWS * 8, STAGE = ROWS * 128;
    constexpr int C = (SLOTS + NTHR - 1) / NTHR;
    constexpr int NK = KD / 64, R = P + 1;
    static_assert(NK >= 2, "K too small");
    __shared__ __attribute__((aligned(16))) unsigned char smem[2 * STAGE];

    const int tid = threadIdx.x, lane = tid & 63;
    const int w = __builtin_amdgcn_readfirstlane(tid >> 6);
    const int wm = w / WN, wn = w % WN;
    constexpr int MPER = (NS / BM) / 8;
    const int xcd = blockIdx.x % 8, idx = blockIdx.x / 8;
    const int m0 = (xcd * MPER + idx % MPER) * BM;
    const int n0 = (idx / MPER) * BN;
    const int fr = lane & 15, fq = lane >> 4;

    const unsigned short* src[C];
    int dst[C];
    bool live[C];
#pragma unroll
    for (int c = 0; c < C; ++c) {
        const int slot = c * NTHR + tid;
        live[c] = __builtin_amdgcn_readfirstlane(slot - lane) < SLOTS;
        const int sl = live[c] ? slot : tid;
        const int row = sl >> 3, ch = sl & 7;
        src[c] = row < AROWS ? A + (size_t)(m0 + row) * KD + ch * 8
                             : Bt + (size_t)min(n0 + row - AROWS, N - 1) * KD + ch * 8;
        dst[c] = row * 128 + ((ch ^ (row & 7)) << 4);
    }
    const unsigned short* asrc[MT];
#pragma unroll
    for (int im = 0; im < MT; ++im) asrc[im] = A + (size_t)(m0 + wm * WTM + im * 16 + fr) * KD + fq * 8;

    uint2 bv[NT], hv[NT][MT];
#pragma unroll
    for (int jn = 0; jn < NT; ++jn) {
        const int n = min(n0 + wn * WTN + jn * 16 + fq * 4, N - 4);
        bv[jn] = *(const uint2*)(bias + n);
        if (EPI == 2) {
#pragma unroll
            for (int im = 0; im < MT; ++im)
                hv[jn][im] = *(const uint2*)(hres + (size_t)(m0 + wm * WTM + im * 16 + fr) * N + n);
        }
    }

    u32x4 rs[R * C];
    bf16x8_t ra[R][2][MT];
#define GEMM_GLOAD(kt_)                                                                                         \
    {                                                                                                           \
        _Pragma("unroll") for (int c = 0; c < C; ++c) if (C * NTHR == SLOTS || live[c])                         \
            rs[((kt_) % R) * C + c] = *(const u32x4*)(src[c] + (kt_) * 64);                                     \
    }
#define GEMM_ALOAD(kt_)                                                                                         \
    if (DIRECT_A) {                                                                                             \
        _Pragma("unroll") for (int kk = 0; kk < 2; ++kk)                                                        \
            _Pragma("unroll") for (int im = 0; im < MT; ++im) ra[(kt_) % R][kk][im] =                           \
                *(const bf16x8_t*)(asrc[im] + (kt_) * 64 + kk * 32);                                            \
    }
#define GEMM_SWRITE(kt_)                                                                                        \
    {                                                                                                           \
        _Pragma("unroll") for (int c = 0; c < C; ++c) if (C * NTHR == SLOTS || live[c])                         \
            *(u32x4*)(smem + ((kt_) & 1) * STAGE + dst[c]) = rs[((kt_) % R) * C + c];                                  \
    }

    f32x4_t acc[NT][MT];
#pragma unroll
    for (int jn = 0; jn < NT; ++jn)
#pragma unroll
        for (int im = 0; im < MT; ++im) acc[jn][im] = f32x4_t{0.f, 0.f, 0.f, 0.f};

    bf16x8_t af[2][2][MT], bfv[2][2][NT];
#define GEMM_LOADF(set_, kt_)                                                                                   \
    {                                                                                                           \
        const unsigned char* sa = smem + ((kt_) & 1) * STAGE;                                                   \
        const unsigned char* sb = sa + AROWS * 128;                                                             \
        _Pragma("unroll") for (int kk = 0; kk < 2; ++kk) {                                                      \
            if (!DIRECT_A) {                                                                                    \
                _Pragma("unroll") for (int im = 0; im < MT; ++im) {                                             \
                    const int row = wm * WTM + im * 16 + fr;                                                    \
                    af[set_][kk][im] = *(const bf16x8_t*)(sa + row * 128 + (((kk * 4 + fq) ^ (row & 7)) << 4)); \
                }                                                                                               \
            }                                                                                                   \
            _Pragma("unroll") for (int jn = 0; jn < NT; ++jn) {                                                 \
                const int row = wn * WTN + jn * 16 + fr;                                                        \
                bfv[set_][kk][jn] = *(const bf16x8_t*)(sb + row * 128 + (((kk * 4 + fq) ^ (row & 7)) << 4));    \
            }                                                                                                   \
        }                                                                                                       \
    }

#pragma unroll
    for (int st = 0; st < R; ++st) {
        GEMM_GLOAD(st)
        GEMM_ALOAD(st)
    }
    GEMM_SWRITE(0)
    if (R < NK) GEMM_GLOAD(R)
    asm volatile("s_waitcnt lgkmcnt(0)\n\ts_barrier" ::: "memory");
    GEMM_LOADF(0, 0)
#pragma unroll
    for (int kt = 0; kt < NK; ++kt) {
        const int cur = kt & 1;
        if (kt + 1 < NK) {
            GEMM_SWRITE(kt + 1)
            if (kt + 1 + R < NK) GEMM_GLOAD(kt + 1 + R)
            asm volatile("s_waitcnt lgkmcnt(0)\n\ts_barrier" ::: "memory");
            GEMM_LOADF(cur ^ 1, kt + 1)
        }
#pragma unroll
        for (int kk = 0; kk < 2; ++kk)
#pragma unroll
            for (int jn = 0; jn < NT; ++jn)
#pragma unroll
                for (int im = 0; im < MT; ++im)
                    acc[jn][im] = __builtin_amdgcn_mfma_f32_16x16x32_bf16(
                        bfv[cur][kk][jn], DIRECT_A ? ra[kt % R][kk][im] : af[cur][kk][im], acc[jn][im], 0, 0, 0);
        if (kt + R < NK) GEMM_ALOAD(kt + R)
    }
#undef GEMM_LOADF
#undef GEMM_SWRITE
#undef GEMM_GLOAD
#undef GEMM_ALOAD

    const float GK0 = -2.0f * 1.4426950408889634f * 0.7978845608028654f;
    const float GK1 = GK0 * 0.044715f;
#pragma unroll
    for (int jn = 0; jn < NT; ++jn) {
        const int n = n0 + wn * WTN + jn * 16 + fq * 4;
        const float b4[4] = {__uint_as_float(bv[jn].x << 16), __uint_as_float(bv[jn].x & 0xffff0000u),
                             __uint_as_float(bv[jn].y << 16), __uint_as_float(bv[jn].y & 0xffff0000u)};
#pragma unroll
        for (int im = 0; im < MT; ++im) {
            const int m = m0 + wm * WTM + im * 16 + fr;
            float r4[4];
#pragma unroll
            for (int e = 0; e < 4; ++e) {
                const float s2 = bfr(bfr(acc[jn][im][e]) + b4[e]);
                if (EPI == 1) {
                    const float t = __builtin_amdgcn_exp2f(s2 * fmaf(GK1, s2 * s2, GK0));
                    r4[e] = s2 * __builtin_amdgcn_rcpf(1.0f + t);
                } else {
                    const unsigned hw = (e < 2) ? hv[jn][im].x : hv[jn][im].y;
                    const float h = (e & 1) ? __uint_as_float(hw & 0xffff0000u) : __uint_as_float(hw << 16);
                    r4[e] = h + s2;
                }
            }
            if (n < N) {
                uint2 o = make_uint2(pack_bf16(r4[0], r4[1]), pack_bf16(r4[2], r4[3]));
                *(uint2*)(outp + (size_t)m * N + n) = o;
            }
        }
    }
}

extern "C" void kernel_launch(void* const* d_in, const int* in_sizes, int n_in, void* d_out, int out_size,
                              void* d_ws, size_t ws_size, hipStream_t stream) {
    const unsigned short* x = (const unsigned short*)d_in[0];
    const unsigned short* wr = (const unsigned short*)d_in[1];
    const unsigned short* wi = (const unsigned short*)d_in[2];
    const unsigned short* w1 = (const unsigned short*)d_in[4];
    const unsigned short* b1 = (const unsigned short*)d_in[5];
    const unsigned short* w2 = (const unsigned short*)d_in[6];
    const unsigned short* b2 = (const unsigned short*)d_in[7];
    unsigned short* out = (unsigned short*)d_out;

    char* ws = (char*)d_ws;
    size_t off = 0;
    auto take = [&](size_t bytes) { char* p = ws + off; off = (off + bytes + 255) & ~(size_t)255; return p; };
    v2f* spec = (v2f*)take((size_t)NKF * NS * 8);
    unsigned* Wt = (unsigned*)take((size_t)NKF * NS * 4);
    unsigned short* hn = (unsigned short*)take((size_t)NS * ND * 2);
    unsigned short* hb = (unsigned short*)take((size_t)NS * ND * 2);
    unsigned short* hid = (unsigned short*)take((size_t)NS * NF * 2);
    unsigned short* w1t = (unsigned short*)take((size_t)NF * ND * 2);
    unsigned short* w2t = (unsigned short*)take((size_t)ND * NF * 2);

    k_stage1<<<FT_BLOCKS + S1_FFT_BLOCKS, 320, 0, stream>>>(x, spec, wr, wi, Wt);
    k_stage2<<<TR2_BLOCKS + NKF, 256, 0, stream>>>(spec, Wt, w1, w1t);
    k_stage3<<<TR3_BLOCKS + NS / 8, 320, 0, stream>>>(spec, x, hb, hn, w2, w2t);
    k_gemm<128, 128, ND, 4, 2, 3, 0, 1><<<(NS / 128) * ((NF + 127) / 128), 512, 0, stream>>>(hn, w1t, NF, b1, nullptr,
                                                                                          hid);
    k_gemm<64, 80, NF, 4, 1, 3, 1, 2><<<(NS / 64) * (ND / 80), 256, 0, stream>>>(hid, w2t, ND, b2, hb, out);
}
```
